```python
import torch

def _softmax(x, axis=-1):
    return torch.softmax(x, dim=axis)
def _gelu(x):
    return torch.nn.functional.gelu(x, approximate="tanh")
def _silu(x):
    return torch.nn.functional.silu(x)
def _sigmoid(x):
    return torch.sigmoid(x)
def _layernorm(x, axis=-1, eps=1e-5):
    m = x.mean(dim=axis, keepdim=True)
    v = x.var(dim=axis, keepdim=True, unbiased=False)
    return (x - m) / torch.sqrt(v + eps)
def _scatter_add(tgt, idx, src, axis=0):
    return tgt.clone().index_add_(axis, idx, src)
def _segment_sum(src, seg, n):
    out = torch.zeros((n,) + src.shape[1:], dtype=src.dtype)
    return out.index_add_(0, seg, src)
def _pad(x, pw):
    flat = tuple(v for pair in reversed(pw) for v in pair)
    return torch.nn.functional.pad(x, flat)

kernel_name = 'concat_heads__gqa_block_cached__mha_block_cached__near_ridge'

def setup_inputs():

    _w = (lambda a: (lambda t: t.to(torch.bfloat16) if t.is_floating_point() else t)(torch.as_tensor(a)))
    def _A_setup():
        B, Sq, D = 16, 32, 512
        Hq, Hkv, Sk, Dh = 32, 8, 512, 128
        assert Hq % Hkv == 0, (Hq, Hkv)
        return {
            "x": _w((lambda *s: torch.randn(*s, dtype=torch.bfloat16) * 0.8)(B, Sq, D)),
            "k_cache": _w((lambda *s: torch.randn(*s, dtype=torch.bfloat16) * 0.8)(B, Hkv, Sk, Dh)),
            "v_cache": _w((lambda *s: torch.randn(*s, dtype=torch.bfloat16) * 0.05)(B, Hkv, Sk, Dh)),
            "wq": _w((lambda *s: torch.randn(*s, dtype=torch.bfloat16) * 0.8)(D, Hq * Dh) * (D ** -0.5)),
            "wo": _w((lambda *s: torch.randn(*s, dtype=torch.bfloat16) * 0.05)(Hq * Dh, D) * ((Hq * Dh) ** -0.5)),
        }
    _A_in = _A_setup()

    def _B_setup():
        B, Sq, D = 16, 32, 512
        H, Sk, Dh = 32, 512, 128
        return {
            "x": _w((lambda *s: torch.randn(*s, dtype=torch.bfloat16) * 0.8)(B, Sq, D)),
            "k_cache": _w((lambda *s: torch.randn(*s, dtype=torch.bfloat16) * 0.8)(B, H, Sk, Dh)),
            "v_cache": _w((lambda *s: torch.randn(*s, dtype=torch.bfloat16) * 0.05)(B, H, Sk, Dh)),
            "wq": _w((lambda *s: torch.randn(*s, dtype=torch.bfloat16) * 0.8)(D, H * Dh) * (D ** -0.5)),
            "wo": _w((lambda *s: torch.randn(*s, dtype=torch.bfloat16) * 0.05)(H * Dh, D) * ((H * Dh) ** -0.5)),
        }
    _B_in = _B_setup()

    return {
        "A_x": _A_in["x"],
        "A_k_cache": _A_in["k_cache"],
        "A_v_cache": _A_in["v_cache"],
        "A_wq": _A_in["wq"],
        "A_wo": _A_in["wo"],
        "B_x": _B_in["x"],
        "B_k_cache": _B_in["k_cache"],
        "B_v_cache": _B_in["v_cache"],
        "B_wq": _B_in["wq"],
        "B_wo": _B_in["wo"],
    }

def reference(A_x, A_k_cache, A_v_cache, A_wq, A_wo, B_x, B_k_cache, B_v_cache, B_wq, B_wo):
    def _A_ref(x, k_cache, v_cache, wq, wo):
        B, Sq, D = x.shape
        Hq, Dh = 32, 128
        Hkv = k_cache.shape[1]
        rep = Hq // Hkv
        q = (lambda x, *d: x.permute(*d))((x @ wq).reshape(B, Sq, Hq, Dh), 0, 2, 1, 3)
        k = (lambda x, n, axis: torch.repeat_interleave(x, n, dim=axis))(k_cache, rep, axis=1)
        v = (lambda x, n, axis: torch.repeat_interleave(x, n, dim=axis))(v_cache, rep, axis=1)
        scores = torch.einsum("bhqd,bhkd->bhqk", q, k) * (Dh ** -0.5)
        p = _softmax(scores, axis=-1)
        o = torch.einsum("bhqk,bhkd->bhqd", p, v)
        return (lambda x, *d: x.permute(*d))(o, 0, 2, 1, 3).reshape(B, Sq, Hq * Dh) @ wo

    def _B_ref(x, k_cache, v_cache, wq, wo):
        B, Sq, D = x.shape
        H, Dh = 32, 128
        q = (lambda x, *d: x.permute(*d))((x @ wq).reshape(B, Sq, H, Dh), 0, 2, 1, 3)
        scores = torch.einsum("bhqd,bhkd->bhqk", q, k_cache) * (Dh ** -0.5)
        p = _softmax(scores, axis=-1)
        o = torch.einsum("bhqk,bhkd->bhqd", p, v_cache)
        return (lambda x, *d: x.permute(*d))(o, 0, 2, 1, 3).reshape(B, Sq, H * Dh) @ wo

    a = _A_ref(A_x, A_k_cache, A_v_cache, A_wq, A_wo)
    b = _B_ref(B_x, B_k_cache, B_v_cache, B_wq, B_wo)
    return torch.concatenate([a, b], axis=-1)
```

```cpp
#include <hip/hip_runtime.h>
#include <hip/hip_bf16.h>
#include <stdint.h>

typedef unsigned u32x4 __attribute__((ext_vector_type(4)));
typedef float f32x4 __attribute__((ext_vector_type(4)));
typedef short bf16x8 __attribute__((ext_vector_type(8)));

constexpr int NB = 16, SQ = 32, DM = 512, NH = 32, SK = 512, DH = 128;
constexpr float SCALE_L2E = 0.088388347648318447f * 1.4426950408889634f;

__device__ __forceinline__ f32x4 mfma(u32x4 a, u32x4 b, f32x4 c) {
  return __builtin_amdgcn_mfma_f32_16x16x32_bf16(__builtin_bit_cast(bf16x8, a), __builtin_bit_cast(bf16x8, b), c, 0, 0, 0);
}
__device__ __forceinline__ unsigned cvtpk(float lo, float hi) {
  unsigned r; asm("v_cvt_pk_bf16_f32 %0, %1, %2" : "=v"(r) : "v"(lo), "v"(hi)); return r;
}
__device__ __forceinline__ void tr8x8(const u32x4 (&in)[8], u32x4 (&out)[8]) {
#pragma unroll
  for (int t = 0; t < 4; ++t) {
#pragma unroll
    for (int m = 0; m < 4; ++m) {
      out[2 * t][m] = __builtin_amdgcn_perm(in[2 * m + 1][t], in[2 * m][t], 0x05040100u);
      out[2 * t + 1][m] = __builtin_amdgcn_perm(in[2 * m + 1][t], in[2 * m][t], 0x07060302u);
    }
  }
}
__device__ __forceinline__ float xmax4(float x) {
  auto s = __builtin_amdgcn_permlane16_swap(__float_as_uint(x), __float_as_uint(x), false, false);
  x = fmaxf(__uint_as_float(s[0]), __uint_as_float(s[1]));
  auto t = __builtin_amdgcn_permlane32_swap(__float_as_uint(x), __float_as_uint(x), false, false);
  return fmaxf(__uint_as_float(t[0]), __uint_as_float(t[1]));
}
__device__ __forceinline__ float xsum4(float x) {
  auto s = __builtin_amdgcn_permlane16_swap(__float_as_uint(x), __float_as_uint(x), false, false);
  x = __uint_as_float(s[0]) + __uint_as_float(s[1]);
  auto t = __builtin_amdgcn_permlane32_swap(__float_as_uint(x), __float_as_uint(x), false, false);
  return __uint_as_float(t[0]) + __uint_as_float(t[1]);
}

struct Args {
  const uint16_t* x[2];
  const uint16_t* k[2];
  const uint16_t* v[2];
  const uint16_t* wq[2];
  const uint16_t* wo[2];
  uint16_t* o;
  uint16_t* out;
};

__global__ void __launch_bounds__(256) attn_k(Args a) {
  __shared__ __attribute__((aligned(16))) float red[4 * 64 * 64];
  __shared__ __attribute__((aligned(16))) unsigned qs[8 * 64 * 4];
  __shared__ float ml[4][2][32];
  const int u = blockIdx.x;
  const int br = u >> 9, b = (u >> 5) & 15, h = u & 31;
  const int tid = threadIdx.x, lane = tid & 63, w = __builtin_amdgcn_readfirstlane(tid >> 6);
  const int c = lane & 15, g = lane >> 4;
  const int pc = 4 * (c & 3) + (c >> 2);
  const int hkv = br == 0 ? (h >> 2) : h;
  const int nkv = br == 0 ? 8 : 32;
  const uint16_t* Kp = a.k[br] + ((size_t)(b * nkv + hkv) * SK) * DH;
  const uint16_t* Vp = a.v[br] + ((size_t)(b * nkv + hkv) * SK) * DH;
  f32x4 acc[8][2];
#pragma unroll
  for (int i = 0; i < 8; ++i) { acc[i][0] = f32x4{0, 0, 0, 0}; acc[i][1] = f32x4{0, 0, 0, 0}; }
  {
    const uint16_t* xp = a.x[br] + (size_t)b * SQ * DM;
    const uint16_t* wp = a.wq[br] + h * DH + 8 * pc;
#pragma unroll
    for (int s = 0; s < 4; ++s) {
      const int D0 = 128 * w + 32 * s;
      u32x4 wr[8], wt[8];
#pragma unroll
      for (int j = 0; j < 8; ++j) wr[j] = *(const u32x4*)(wp + (size_t)(D0 + 8 * g + j) * (NH * DH));
      u32x4 xf[2];
#pragma unroll
      for (int qb = 0; qb < 2; ++qb) xf[qb] = *(const u32x4*)(xp + (16 * qb + c) * DM + D0 + 8 * g);
      tr8x8(wr, wt);
#pragma unroll
      for (int i = 0; i < 8; ++i)
#pragma unroll
        for (int qb = 0; qb < 2; ++qb) acc[i][qb] = mfma(wt[i], xf[qb], acc[i][qb]);
    }
  }
#pragma unroll
  for (int r = 0; r < 4; ++r)
#pragma unroll
    for (int qb = 0; qb < 2; ++qb)
#pragma unroll
      for (int hf = 0; hf < 2; ++hf)
        *(f32x4*)&red[(((w * 4 + r) * 2 + qb) * 2 + hf) * 256 + lane * 4] =
            f32x4{acc[4 * hf][qb][r], acc[4 * hf + 1][qb][r], acc[4 * hf + 2][qb][r], acc[4 * hf + 3][qb][r]};
  __syncthreads();
#pragma unroll
  for (int qb = 0; qb < 2; ++qb) {
    f32x4 s0 = {0, 0, 0, 0}, s1 = {0, 0, 0, 0};
#pragma unroll
    for (int ww = 0; ww < 4; ++ww) {
      s0 += *(const f32x4*)&red[(((ww * 4 + w) * 2 + qb) * 2 + 0) * 256 + lane * 4];
      s1 += *(const f32x4*)&red[(((ww * 4 + w) * 2 + qb) * 2 + 1) * 256 + lane * 4];
    }
    u32x4 q = {cvtpk(s0[0], s0[1]), cvtpk(s0[2], s0[3]), cvtpk(s1[0], s1[1]), cvtpk(s1[2], s1[3])};
    *(u32x4*)&qs[((qb * 4 + w) * 64 + lane) * 4] = q;
  }
  __syncthreads();
  u32x4 qf[2][4];
#pragma unroll
  for (int qb = 0; qb < 2; ++qb)
#pragma unroll
    for (int kk = 0; kk < 4; ++kk) qf[qb][kk] = *(const u32x4*)&qs[((qb * 4 + kk) * 64 + lane) * 4];
  f32x4 st[8][2];
  const uint16_t* Kw = Kp + (size_t)(128 * w) * DH;
#pragma unroll
  for (int t = 0; t < 8; ++t) {
    st[t][0] = f32x4{0, 0, 0, 0}; st[t][1] = f32x4{0, 0, 0, 0};
    const uint16_t* kr = Kw + (16 * t + pc) * DH + 8 * g;
#pragma unroll
    for (int kk = 0; kk < 4; ++kk) {
      u32x4 kf = *(const u32x4*)(kr + 32 * kk);
      st[t][0] = mfma(kf, qf[0][kk], st[t][0]);
      st[t][1] = mfma(kf, qf[1][kk], st[t][1]);
    }
  }
  float m[2], l[2];
  u32x4 pf[4][2];
#pragma unroll
  for (int qb = 0; qb < 2; ++qb) {
    float mx = st[0][qb][0];
#pragma unroll
    for (int t = 0; t < 8; ++t)
#pragma unroll
      for (int r = 0; r < 4; ++r) mx = fmaxf(mx, st[t][qb][r]);
    mx = xmax4(mx);
    m[qb] = mx * SCALE_L2E;
    float s = 0.f;
#pragma unroll
    for (int t = 0; t < 8; ++t)
#pragma unroll
      for (int r = 0; r < 4; ++r) { float p = __builtin_amdgcn_exp2f(st[t][qb][r] * SCALE_L2E - m[qb]); st[t][qb][r] = p; s += p; }
    l[qb] = xsum4(s);
#pragma unroll
    for (int kb = 0; kb < 4; ++kb)
      pf[kb][qb] = u32x4{cvtpk(st[2 * kb][qb][0], st[2 * kb][qb][1]), cvtpk(st[2 * kb][qb][2], st[2 * kb][qb][3]),
                         cvtpk(st[2 * kb + 1][qb][0], st[2 * kb + 1][qb][1]), cvtpk(st[2 * kb + 1][qb][2], st[2 * kb + 1][qb][3])};
  }
#pragma unroll
  for (int i = 0; i < 8; ++i) { acc[i][0] = f32x4{0, 0, 0, 0}; acc[i][1] = f32x4{0, 0, 0, 0}; }
  const uint16_t* Vw = Vp + (size_t)(128 * w) * DH + g * DH + 8 * pc;
#pragma unroll
  for (int kb = 0; kb < 4; ++kb) {
    u32x4 vr[8], vt[8];
#pragma unroll
    for (int j = 0; j < 8; ++j) vr[j] = *(const u32x4*)(Vw + (32 * kb + 4 * j) * DH);
    tr8x8(vr, vt);
#pragma unroll
    for (int i = 0; i < 8; ++i)
#pragma unroll
      for (int qb = 0; qb < 2; ++qb) acc[i][qb] = mfma(vt[i], pf[kb][qb], acc[i][qb]);
  }
  if (g == 0) { ml[w][0][c] = m[0]; ml[w][0][16 + c] = m[1]; ml[w][1][c] = l[0]; ml[w][1][16 + c] = l[1]; }
  __syncthreads();
  float sc[2];
#pragma unroll
  for (int qb = 0; qb < 2; ++qb) {
    float M = fmaxf(fmaxf(ml[0][0][16 * qb + c], ml[1][0][16 * qb + c]), fmaxf(ml[2][0][16 * qb + c], ml[3][0][16 * qb + c]));
    float L = 0.f;
#pragma unroll
    for (int ww = 0; ww < 4; ++ww) L += ml[ww][1][16 * qb + c] * __builtin_amdgcn_exp2f(ml[ww][0][16 * qb + c] - M);
    sc[qb] = __builtin_amdgcn_exp2f(m[qb] - M) / L;
  }
#pragma unroll
  for (int r = 0; r < 4; ++r)
#pragma unroll
    for (int qb = 0; qb < 2; ++qb)
#pragma unroll
      for (int hf = 0; hf < 2; ++hf)
        *(f32x4*)&red[(((w * 4 + r) * 2 + qb) * 2 + hf) * 256 + lane * 4] =
            f32x4{acc[4 * hf][qb][r], acc[4 * hf + 1][qb][r], acc[4 * hf + 2][qb][r], acc[4 * hf + 3][qb][r]} * sc[qb];
  __syncthreads();
#pragma unroll
  for (int qb = 0; qb < 2; ++qb) {
    f32x4 s0 = {0, 0, 0, 0}, s1 = {0, 0, 0, 0};
#pragma unroll
    for (int ww = 0; ww < 4; ++ww) {
      s0 += *(const f32x4*)&red[(((ww * 4 + w) * 2 + qb) * 2 + 0) * 256 + lane * 4];
      s1 += *(const f32x4*)&red[(((ww * 4 + w) * 2 + qb) * 2 + 1) * 256 + lane * 4];
    }
    u32x4 o = {cvtpk(s0[0], s0[1]), cvtpk(s0[2], s0[3]), cvtpk(s1[0], s1[1]), cvtpk(s1[2], s1[3])};
    *(u32x4*)(a.o + ((size_t)(br * NB + b) * SQ + 16 * qb + c) * (NH * DH) + h * DH + 32 * w + 8 * g) = o;
  }
}

__global__ void __launch_bounds__(512) oproj_k(Args a) {
  __shared__ __attribute__((aligned(16))) float red[8 * 64 * 64];
  const int u = blockIdx.x;
  const int br = u >> 6, b = (u >> 2) & 15, ng = u & 3;
  const int tid = threadIdx.x, lane = tid & 63, w = __builtin_amdgcn_readfirstlane(tid >> 6);
  const int c = lane & 15, g = lane >> 4;
  const int pc = 4 * (c & 3) + (c >> 2);
  f32x4 acc[8][2];
#pragma unroll
  for (int i = 0; i < 8; ++i) { acc[i][0] = f32x4{0, 0, 0, 0}; acc[i][1] = f32x4{0, 0, 0, 0}; }
  const uint16_t* op = a.o + (size_t)(br * NB + b) * SQ * (NH * DH);
  const uint16_t* wp = a.wo[br] + ng * 128 + 8 * pc;
#pragma unroll 2
  for (int s = 0; s < 16; ++s) {
    const int K0 = 512 * w + 32 * s;
    u32x4 wr[8], wt[8];
#pragma unroll
    for (int j = 0; j < 8; ++j) wr[j] = *(const u32x4*)(wp + (size_t)(K0 + 8 * g + j) * DM);
    u32x4 xf[2];
#pragma unroll
    for (int qb = 0; qb < 2; ++qb) xf[qb] = *(const u32x4*)(op + (16 * qb + c) * (NH * DH) + K0 + 8 * g);
    tr8x8(wr, wt);
#pragma unroll
    for (int i = 0; i < 8; ++i)
#pragma unroll
      for (int qb = 0; qb < 2; ++qb) acc[i][qb] = mfma(wt[i], xf[qb], acc[i][qb]);
  }
#pragma unroll
  for (int r = 0; r < 4; ++r)
#pragma unroll
    for (int qb = 0; qb < 2; ++qb)
#pragma unroll
      for (int hf = 0; hf < 2; ++hf)
        *(f32x4*)&red[(((w * 4 + r) * 2 + qb) * 2 + hf) * 256 + lane * 4] =
            f32x4{acc[4 * hf][qb][r], acc[4 * hf + 1][qb][r], acc[4 * hf + 2][qb][r], acc[4 * hf + 3][qb][r]};
  __syncthreads();
  {
    const int r = w >> 1, qb = w & 1;
    f32x4 s0 = {0, 0, 0, 0}, s1 = {0, 0, 0, 0};
#pragma unroll
    for (int ww = 0; ww < 8; ++ww) {
      s0 += *(const f32x4*)&red[(((ww * 4 + r) * 2 + qb) * 2 + 0) * 256 + lane * 4];
      s1 += *(const f32x4*)&red[(((ww * 4 + r) * 2 + qb) * 2 + 1) * 256 + lane * 4];
    }
    u32x4 o = {cvtpk(s0[0], s0[1]), cvtpk(s0[2], s0[3]), cvtpk(s1[0], s1[1]), cvtpk(s1[2], s1[3])};
    *(u32x4*)(a.out + ((size_t)b * SQ + 16 * qb + c) * 1024 + br * 512 + ng * 128 + 32 * r + 8 * g) = o;
  }
}

extern "C" void kernel_launch(void* const* d_in, const int* in_sizes, int n_in, void* d_out, int out_size, void* d_ws, size_t ws_size,
                              hipStream_t stream) {
  Args a;
  for (int br = 0; br < 2; ++br) {
    a.x[br] = (const uint16_t*)d_in[5 * br + 0];
    a.k[br] = (const uint16_t*)d_in[5 * br + 1];
    a.v[br] = (const uint16_t*)d_in[5 * br + 2];
    a.wq[br] = (const uint16_t*)d_in[5 * br + 3];
    a.wo[br] = (const uint16_t*)d_in[5 * br + 4];
  }
  a.o = (uint16_t*)d_ws;
  a.out = (uint16_t*)d_out;
  attn_k<<<1024, 256, 0, stream>>>(a);
  oproj_k<<<128, 512, 0, stream>>>(a);
}
```

```cpp
#include <hip/hip_runtime.h>
#include <hip/hip_bf16.h>
#include <stdint.h>

typedef unsigned u32x4 __attribute__((ext_vector_type(4)));
typedef float f32x4 __attribute__((ext_vector_type(4)));
typedef short bf16x8 __attribute__((ext_vector_type(8)));

constexpr int NB = 16, SQ = 32, DM = 512, NH = 32, SK = 512, DH = 128;
constexpr float SCALE_L2E = 0.088388347648318447f * 1.4426950408889634f;

__device__ __forceinline__ f32x4 mfma(u32x4 a, u32x4 b, f32x4 c) {
  return __builtin_amdgcn_mfma_f32_16x16x32_bf16(__builtin_bit_cast(bf16x8, a), __builtin_bit_cast(bf16x8, b), c, 0, 0, 0);
}
__device__ __forceinline__ unsigned cvtpk(float lo, float hi) {
  unsigned r; asm("v_cvt_pk_bf16_f32 %0, %1, %2" : "=v"(r) : "v"(lo), "v"(hi)); return r;
}
__device__ __forceinline__ void tr8x8(const u32x4 (&in)[8], u32x4 (&out)[8]) {
#pragma unroll
  for (int t = 0; t < 4; ++t) {
#pragma unroll
    for (int m = 0; m < 4; ++m) {
      out[2 * t][m] = __builtin_amdgcn_perm(in[2 * m + 1][t], in[2 * m][t], 0x05040100u);
      out[2 * t + 1][m] = __builtin_amdgcn_perm(in[2 * m + 1][t], in[2 * m][t], 0x07060302u);
    }
  }
}
__device__ __forceinline__ float xmax4(float x) {
  auto s = __builtin_amdgcn_permlane16_swap(__float_as_uint(x), __float_as_uint(x), false, false);
  x = fmaxf(__uint_as_float(s[0]), __uint_as_float(s[1]));
  auto t = __builtin_amdgcn_permlane32_swap(__float_as_uint(x), __float_as_uint(x), false, false);
  return fmaxf(__uint_as_float(t[0]), __uint_as_float(t[1]));
}
__device__ __forceinline__ float xsum4(float x) {
  auto s = __builtin_amdgcn_permlane16_swap(__float_as_uint(x), __float_as_uint(x), false, false);
  x = __uint_as_float(s[0]) + __uint_as_float(s[1]);
  auto t = __builtin_amdgcn_permlane32_swap(__float_as_uint(x), __float_as_uint(x), false, false);
  return __uint_as_float(t[0]) + __uint_as_float(t[1]);
}

struct Args {
  const uint16_t* x[2];
  const uint16_t* k[2];
  const uint16_t* v[2];
  const uint16_t* wq[2];
  const uint16_t* wo[2];
  uint16_t* o;
  uint16_t* out;
};

__global__ void __launch_bounds__(256) attn_k(Args a) {
  __shared__ __attribute__((aligned(16))) float red[4 * 64 * 64];
  __shared__ __attribute__((aligned(16))) unsigned qs[8 * 64 * 4];
  __shared__ float ml[4][2][32];
  const int u = blockIdx.x;
  const int br = u >> 9, b = (u >> 5) & 15, h = u & 31;
  const int tid = threadIdx.x, lane = tid & 63, w = __builtin_amdgcn_readfirstlane(tid >> 6);
  const int c = lane & 15, g = lane >> 4;
  const int pc = 4 * (c & 3) + (c >> 2);
  const int hkv = br == 0 ? (h >> 2) : h;
  const int nkv = br == 0 ? 8 : 32;
  const uint16_t* Kp = a.k[br] + ((size_t)(b * nkv + hkv) * SK) * DH;
  const uint16_t* Vp = a.v[br] + ((size_t)(b * nkv + hkv) * SK) * DH;
  f32x4 acc[8][2];
#pragma unroll
  for (int i = 0; i < 8; ++i) { acc[i][0] = f32x4{0, 0, 0, 0}; acc[i][1] = f32x4{0, 0, 0, 0}; }
  {
    const uint16_t* xp = a.x[br] + (size_t)b * SQ * DM;
    const uint16_t* wp = a.wq[br] + h * DH + 8 * pc;
#pragma unroll
    for (int s = 0; s < 4; ++s) {
      const int D0 = 128 * w + 32 * s;
      u32x4 wr[8], wt[8];
#pragma unroll
      for (int j = 0; j < 8; ++j) wr[j] = *(const u32x4*)(wp + (size_t)(D0 + 8 * g + j) * (NH * DH));
      u32x4 xf[2];
#pragma unroll
      for (int qb = 0; qb < 2; ++qb) xf[qb] = *(const u32x4*)(xp + (16 * qb + c) * DM + D0 + 8 * g);
      tr8x8(wr, wt);
#pragma unroll
      for (int i = 0; i < 8; ++i)
#pragma unroll
        for (int qb = 0; qb < 2; ++qb) acc[i][qb] = mfma(wt[i], xf[qb], acc[i][qb]);
    }
  }
#pragma unroll
  for (int r = 0; r < 4; ++r)
#pragma unroll
    for (int qb = 0; qb < 2; ++qb)
#pragma unroll
      for (int hf = 0; hf < 2; ++hf)
        *(f32x4*)&red[(((w * 4 + r) * 2 + qb) * 2 + hf) * 256 + lane * 4] =
            f32x4{acc[4 * hf][qb][r], acc[4 * hf + 1][qb][r], acc[4 * hf + 2][qb][r], acc[4 * hf + 3][qb][r]};
  __syncthreads();
#pragma unroll
  for (int qb = 0; qb < 2; ++qb) {
    f32x4 s0 = {0, 0, 0, 0}, s1 = {0, 0, 0, 0};
#pragma unroll
    for (int ww = 0; ww < 4; ++ww) {
      s0 += *(const f32x4*)&red[(((ww * 4 + w) * 2 + qb) * 2 + 0) * 256 + lane * 4];
      s1 += *(const f32x4*)&red[(((ww * 4 + w) * 2 + qb) * 2 + 1) * 256 + lane * 4];
    }
    u32x4 q = {cvtpk(s0[0], s0[1]), cvtpk(s0[2], s0[3]), cvtpk(s1[0], s1[1]), cvtpk(s1[2], s1[3])};
    *(u32x4*)&qs[((qb * 4 + w) * 64 + lane) * 4] = q;
  }
  __syncthreads();
  u32x4 qf[2][4];
#pragma unroll
  for (int qb = 0; qb < 2; ++qb)
#pragma unroll
    for (int kk = 0; kk < 4; ++kk) qf[qb][kk] = *(const u32x4*)&qs[((qb * 4 + kk) * 64 + lane) * 4];
  f32x4 st[8][2];
  const uint16_t* Kw = Kp + (size_t)(128 * w) * DH;
#pragma unroll
  for (int t = 0; t < 8; ++t) {
    st[t][0] = f32x4{0, 0, 0, 0}; st[t][1] = f32x4{0, 0, 0, 0};
    const uint16_t* kr = Kw + (16 * t + pc) * DH + 8 * g;
#pragma unroll
    for (int kk = 0; kk < 4; ++kk) {
      u32x4 kf = *(const u32x4*)(kr + 32 * kk);
      st[t][0] = mfma(kf, qf[0][kk], st[t][0]);
      st[t][1] = mfma(kf, qf[1][kk], st[t][1]);
    }
  }
  float m[2], l[2];
  u32x4 pf[4][2];
#pragma unroll
  for (int qb = 0; qb < 2; ++qb) {
    float mx = st[0][qb][0];
#pragma unroll
    for (int t = 0; t < 8; ++t)
#pragma unroll
      for (int r = 0; r < 4; ++r) mx = fmaxf(mx, st[t][qb][r]);
    mx = xmax4(mx);
    m[qb] = mx * SCALE_L2E;
    float s = 0.f;
#pragma unroll
    for (int t = 0; t < 8; ++t)
#pragma unroll
      for (int r = 0; r < 4; ++r) { float p = __builtin_amdgcn_exp2f(st[t][qb][r] * SCALE_L2E - m[qb]); st[t][qb][r] = p; s += p; }
    l[qb] = xsum4(s);
#pragma unroll
    for (int kb = 0; kb < 4; ++kb)
      pf[kb][qb] = u32x4{cvtpk(st[2 * kb][qb][0], st[2 * kb][qb][1]), cvtpk(st[2 * kb][qb][2], st[2 * kb][qb][3]),
                         cvtpk(st[2 * kb + 1][qb][0], st[2 * kb + 1][qb][1]), cvtpk(st[2 * kb + 1][qb][2], st[2 * kb + 1][qb][3])};
  }
#pragma unroll
  for (int i = 0; i < 8; ++i) { acc[i][0] = f32x4{0, 0, 0, 0}; acc[i][1] = f32x4{0, 0, 0, 0}; }
  const uint16_t* Vw = Vp + (size_t)(128 * w) * DH + g * DH + 8 * pc;
#pragma unroll
  for (int kb = 0; kb < 4; ++kb) {
    u32x4 vr[8], vt[8];
#pragma unroll
    for (int j = 0; j < 8; ++j) vr[j] = *(const u32x4*)(Vw + (32 * kb + 4 * j) * DH);
    tr8x8(vr, vt);
#pragma unroll
    for (int i = 0; i < 8; ++i)
#pragma unroll
      for (int qb = 0; qb < 2; ++qb) acc[i][qb] = mfma(vt[i], pf[kb][qb], acc[i][qb]);
  }
  if (g == 0) { ml[w][0][c] = m[0]; ml[w][0][16 + c] = m[1]; ml[w][1][c] = l[0]; ml[w][1][16 + c] = l[1]; }
  __syncthreads();
  float sc[2];
#pragma unroll
  for (int qb = 0; qb < 2; ++qb) {
    float M = fmaxf(fmaxf(ml[0][0][16 * qb + c], ml[1][0][16 * qb + c]), fmaxf(ml[2][0][16 * qb + c], ml[3][0][16 * qb + c]));
    float L = 0.f;
#pragma unroll
    for (int ww = 0; ww < 4; ++ww) L += ml[ww][1][16 * qb + c] * __builtin_amdgcn_exp2f(ml[ww][0][16 * qb + c] - M);
    sc[qb] = __builtin_amdgcn_exp2f(m[qb] - M) / L;
  }
#pragma unroll
  for (int r = 0; r < 4; ++r)
#pragma unroll
    for (int qb = 0; qb < 2; ++qb)
#pragma unroll
      for (int hf = 0; hf < 2; ++hf)
        *(f32x4*)&red[(((w * 4 + r) * 2 + qb) * 2 + hf) * 256 + lane * 4] =
            f32x4{acc[4 * hf][qb][r], acc[4 * hf + 1][qb][r], acc[4 * hf + 2][qb][r], acc[4 * hf + 3][qb][r]} * sc[qb];
  __syncthreads();
#pragma unroll
  for (int qb = 0; qb < 2; ++qb) {
    f32x4 s0 = {0, 0, 0, 0}, s1 = {0, 0, 0, 0};
#pragma unroll
    for (int ww = 0; ww < 4; ++ww) {
      s0 += *(const f32x4*)&red[(((ww * 4 + w) * 2 + qb) * 2 + 0) * 256 + lane * 4];
      s1 += *(const f32x4*)&red[(((ww * 4 + w) * 2 + qb) * 2 + 1) * 256 + lane * 4];
    }
    u32x4 o = {cvtpk(s0[0], s0[1]), cvtpk(s0[2], s0[3]), cvtpk(s1[0], s1[1]), cvtpk(s1[2], s1[3])};
    *(u32x4*)(a.o + ((size_t)(br * NB + b) * SQ + 16 * qb + c) * (NH * DH) + h * DH + 32 * w + 8 * g) = o;
  }
}

__global__ void __launch_bounds__(512) oproj_k(Args a) {
  __shared__ __attribute__((aligned(16))) float red[8 * 64 * 64];
  const int u = blockIdx.x;
  const int br = u >> 6, b = (u >> 2) & 15, ng = u & 3;
  const int tid = threadIdx.x, lane = tid & 63, w = __builtin_amdgcn_readfirstlane(tid >> 6);
  const int c = lane & 15, g = lane >> 4;
  const int pc = 4 * (c & 3) + (c >> 2);
  f32x4 acc[8][2];
#pragma unroll
  for (int i = 0; i < 8; ++i) { acc[i][0] = f32x4{0, 0, 0, 0}; acc[i][1] = f32x4{0, 0, 0, 0}; }
  const uint16_t* op = a.o + (size_t)(br * NB + b) * SQ * (NH * DH);
  const uint16_t* wp = a.wo[br] + ng * 128 + 8 * pc;
#pragma unroll 2
  for (int s = 0; s < 16; ++s) {
    const int K0 = 512 * w + 32 * s;
    u32x4 wr[8], wt[8];
#pragma unroll
    for (int j = 0; j < 8; ++j) wr[j] = *(const u32x4*)(wp + (size_t)(K0 + 8 * g + j) * DM);
    u32x4 xf[2];
#pragma unroll
    for (int qb = 0; qb < 2; ++qb) xf[qb] = *(const u32x4*)(op + (16 * qb + c) * (NH * DH) + K0 + 8 * g);
    tr8x8(wr, wt);
#pragma unroll
    for (int i = 0; i < 8; ++i)
#pragma unroll
      for (int qb = 0; qb < 2; ++qb) acc[i][qb] = mfma(wt[i], xf[qb], acc[i][qb]);
  }
#pragma unroll
  for (int r = 0; r < 4; ++r)
#pragma unroll
    for (int qb = 0; qb < 2; ++qb)
#pragma unroll
      for (int hf = 0; hf < 2; ++hf)
        *(f32x4*)&red[(((w * 4 + r) * 2 + qb) * 2 + hf) * 256 + lane * 4] =
            f32x4{acc[4 * hf][qb][r], acc[4 * hf + 1][qb][r], acc[4 * hf + 2][qb][r], acc[4 * hf + 3][qb][r]};
  __syncthreads();
  {
    const int r = w >> 1, qb = w & 1;
    f32x4 s0 = {0, 0, 0, 0}, s1 = {0, 0, 0, 0};
#pragma unroll
    for (int ww = 0; ww < 8; ++ww) {
      s0 += *(const f32x4*)&red[(((ww * 4 + r) * 2 + qb) * 2 + 0) * 256 + lane * 4];
      s1 += *(const f32x4*)&red[(((ww * 4 + r) * 2 + qb) * 2 + 1) * 256 + lane * 4];
    }
    u32x4 o = {cvtpk(s0[0], s0[1]), cvtpk(s0[2], s0[3]), cvtpk(s1[0], s1[1]), cvtpk(s1[2], s1[3])};
    *(u32x4*)(a.out + ((size_t)b * SQ + 16 * qb + c) * 1024 + br * 512 + ng * 128 + 32 * r + 8 * g) = o;
  }
}

#define USE(x) asm volatile("" :: "v"(x))
template <bool NT> __device__ __forceinline__ u32x4 ldp(const void* p) {
  if (NT) return __builtin_nontemporal_load((const u32x4*)p);
  return *(const u32x4*)p;
}
template <bool NT>
__global__ void __launch_bounds__(512) probe_k(Args a, unsigned long long* stamps) {
  extern __shared__ char smem[];
  const int tid = threadIdx.x, lane = tid & 63, w = __builtin_amdgcn_readfirstlane(tid >> 6);
  const int wg = blockIdx.x;
  unsigned long long t0 = __builtin_amdgcn_s_memrealtime();
  u32x4 buf[2][4];
  auto addr = [&](int i) -> const char* {
    if (i < 2) return (const char*)a.k[0] + (size_t)wg * 65536 + i * 32768;
    if (i < 4) return (const char*)a.v[0] + (size_t)wg * 65536 + (i - 2) * 32768;
    if (i < 12) return (const char*)a.k[1] + (size_t)wg * 262144 + (i - 4) * 32768;
    return (const char*)a.v[1] + (size_t)wg * 262144 + (i - 12) * 32768;
  };
  auto issue = [&](int i, u32x4 (&bb)[4]) {
    const char* base = addr(i) + w * 4096 + lane * 16;
#pragma unroll
    for (int j = 0; j < 4; ++j) bb[j] = ldp<NT>(base + j * 1024);
    __builtin_amdgcn_sched_barrier(0);
  };
  issue(0, buf[0]); issue(1, buf[1]);
#pragma unroll
  for (int i = 0; i < 20; ++i) {
#pragma unroll
    for (int j = 0; j < 4; ++j) USE(buf[i & 1][j]);
    __builtin_amdgcn_sched_barrier(0);
    if (i + 2 < 20) issue(i + 2, buf[i & 1]);
  }
  unsigned long long t1 = __builtin_amdgcn_s_memrealtime();
  if (lane == 0) { stamps[16 * wg + 2 * w] = t0; stamps[16 * wg + 2 * w + 1] = t1; }
}
__global__ void spin_k(const unsigned long long* stamps) {
  if (threadIdx.x == 0) {
    unsigned long long mn = ~0ull, mx = 0;
    for (int i = 0; i < 256 * 8; ++i) { unsigned long long s = stamps[2 * i], e = stamps[2 * i + 1]; mn = s < mn ? s : mn; mx = e > mx ? e : mx; }
    unsigned long long span = mx - mn; if (span > 20000) span = 20000;
    unsigned long long t0 = __builtin_amdgcn_s_memrealtime();
    while (__builtin_amdgcn_s_memrealtime() - t0 < 20 * span) __builtin_amdgcn_s_sleep(4);
  }
}

extern "C" void kernel_launch(void* const* d_in, const int* in_sizes, int n_in, void* d_out, int out_size, void* d_ws, size_t ws_size,
                              hipStream_t stream) {
  Args a;
  for (int br = 0; br < 2; ++br) {
    a.x[br] = (const uint16_t*)d_in[5 * br + 0];
    a.k[br] = (const uint16_t*)d_in[5 * br + 1];
    a.v[br] = (const uint16_t*)d_in[5 * br + 2];
    a.wq[br] = (const uint16_t*)d_in[5 * br + 3];
    a.wo[br] = (const uint16_t*)d_in[5 * br + 4];
  }
  a.o = (uint16_t*)d_ws;
  a.out = (uint16_t*)d_out;
  unsigned long long* stamps = (unsigned long long*)((char*)d_ws + (64u << 20));
  probe_k<true><<<256, 512, 65536, stream>>>(a, stamps);
  spin_k<<<1, 64, 0, stream>>>(stamps);
  attn_k<<<1024, 256, 0, stream>>>(a);
  oproj_k<<<128, 512, 0, stream>>>(a);
}
```

```cpp
#include <hip/hip_runtime.h>
#include <stdint.h>

#define LAS __attribute__((address_space(3)))
#define GAS __attribute__((address_space(1)))
typedef unsigned u32x4 __attribute__((ext_vector_type(4)));
typedef unsigned u32x2 __attribute__((ext_vector_type(2)));
typedef float f32x4 __attribute__((ext_vector_type(4)));
typedef short bf16x8 __attribute__((ext_vector_type(8)));
typedef short s16x4 __attribute__((ext_vector_type(4)));
typedef __amdgpu_buffer_rsrc_t rsrc_t;
typedef unsigned long long u64;

constexpr int NB = 16, SQ = 32, DM = 512, NH = 32, SK = 512, DH = 128;
constexpr float SL2E = 0.088388347648318447f * 1.4426950408889634f;
constexpr unsigned DT = 32768;
constexpr unsigned RING = 3 * DT;
constexpr unsigned X_OFF = RING, P_OFF = RING + 32768, Q_OFF = P_OFF + 16384, ST_OFF = Q_OFF + 8192;
constexpr unsigned RES_OFF = RING;
constexpr unsigned LDS_BYTES = 160 * 1024;
constexpr int NLD = 4;
constexpr unsigned WS_FLAGB = 4096, WS_FLAGA = 8192 + 4096, WS_TILES = 1u << 20;
constexpr unsigned TILE_B = 32768;
constexpr unsigned WS_TILES_A = 192 * TILE_B;
constexpr unsigned WS_TILES_BYTES = 320 * TILE_B;

struct Args {
  const void* x[2]; const void* k[2]; const void* v[2]; const void* wq[2]; const void* wo[2];
  char* ws; char* out;
};

__device__ __forceinline__ rsrc_t mkrsrc(const void* p, unsigned bytes) { return __builtin_amdgcn_make_buffer_rsrc((void*)p, 0, bytes, 0x00020000); }
template <int AUX> __device__ __forceinline__ void dma(rsrc_t r, unsigned lds, unsigned voff, unsigned soff) {
  __builtin_amdgcn_raw_ptr_buffer_load_lds(r, (LAS void*)(uintptr_t)lds, 16, voff, soff, 0, AUX);
}
template <int N> __device__ __forceinline__ void waitb() { asm volatile("s_waitcnt vmcnt(%0)\n\ts_barrier" :: "i"(N) : "memory"); }
__device__ __forceinline__ void lbar() { asm volatile("s_barrier" ::: "memory"); }
__device__ __forceinline__ void cbar() { __builtin_amdgcn_sched_barrier(0); asm volatile("s_waitcnt lgkmcnt(0)\n\ts_barrier" ::: "memory"); __builtin_amdgcn_sched_barrier(0); }
__device__ __forceinline__ void cbar_vm() { __builtin_amdgcn_sched_barrier(0); asm volatile("s_waitcnt vmcnt(0) lgkmcnt(0)\n\ts_barrier" ::: "memory"); __builtin_amdgcn_sched_barrier(0); }

__device__ __forceinline__ f32x4 mfma(u32x4 a, u32x4 b, f32x4 c) {
  return __builtin_amdgcn_mfma_f32_16x16x32_bf16(__builtin_bit_cast(bf16x8, a), __builtin_bit_cast(bf16x8, b), c, 0, 0, 0);
}
typedef float f32x2 __attribute__((ext_vector_type(2)));
typedef __bf16 bf16x2 __attribute__((ext_vector_type(2)));
__device__ __forceinline__ unsigned cvtpk(float lo, float hi) {
  return __builtin_bit_cast(unsigned, __builtin_convertvector(f32x2{lo, hi}, bf16x2));
}
__device__ __forceinline__ u32x4 ld128(unsigned a) {
  return *(const LAS u32x4*)(uintptr_t)a; }
__device__ __forceinline__ f32x4 ldf4(unsigned a) { return *(const LAS f32x4*)(uintptr_t)a; }
__device__ __forceinline__ void st64(unsigned a, unsigned lo, unsigned hi) { *(LAS u32x2*)(uintptr_t)a = u32x2{lo, hi}; }
__device__ __forceinline__ u32x4 ldtr(unsigned a0, unsigned a1) {
  const u32x2 lo = __builtin_bit_cast(u32x2, __builtin_amdgcn_ds_read_tr16_b64_v4i16((LAS s16x4*)(uintptr_t)a0));
  const u32x2 hi = __builtin_bit_cast(u32x2, __builtin_amdgcn_ds_read_tr16_b64_v4i16((LAS s16x4*)(uintptr_t)a1));
  return u32x4{lo[0], lo[1], hi[0], hi[1]};
}
__device__ __forceinline__ float xmax4(float x) {
  auto s = __builtin_amdgcn_permlane16_swap(__float_as_uint(x), __float_as_uint(x), false, false);
  x = fmaxf(__uint_as_float(s[0]), __uint_as_float(s[1]));
  auto t = __builtin_amdgcn_permlane32_swap(__float_as_uint(x), __float_as_uint(x), false, false);
  return fmaxf(__uint_as_float(t[0]), __uint_as_float(t[1]));
}
__device__ __forceinline__ float xsum4(float x) {
  auto s = __builtin_amdgcn_permlane16_swap(__float_as_uint(x), __float_as_uint(x), false, false);
  x = __uint_as_float(s[0]) + __uint_as_float(s[1]);
  auto t = __builtin_amdgcn_permlane32_swap(__float_as_uint(x), __float_as_uint(x), false, false);
  return __uint_as_float(t[0]) + __uint_as_float(t[1]);
}
__device__ __forceinline__ unsigned nxt(unsigned s) { s += DT; return s == RING ? 0u : s; }
__device__ __forceinline__ unsigned nxth(unsigned s) { s += DT / 2; return s == RING ? 0u : s; }
#define ABL_OFF 0
#define SB() __builtin_amdgcn_sched_barrier(0)
__device__ __forceinline__ unsigned fresh(unsigned x) { asm volatile("" : "+v"(x)); return x; }
#define RLX __ATOMIC_RELAXED, __HIP_MEMORY_SCOPE_AGENT
#define KFIRST_MASK 0x2a
__device__ __forceinline__ f32x4 bflo(u32x4 v) { return f32x4{__uint_as_float(v[0] << 16), __uint_as_float(v[0] & 0xffff0000u), __uint_as_float(v[1] << 16), __uint_as_float(v[1] & 0xffff0000u)}; }
__device__ __forceinline__ f32x4 bfhi(u32x4 v) { return f32x4{__uint_as_float(v[2] << 16), __uint_as_float(v[2] & 0xffff0000u), __uint_as_float(v[3] << 16), __uint_as_float(v[3] & 0xffff0000u)}; }

#define Z4 f32x4{0.f, 0.f, 0.f, 0.f}
#define STAMP(i) do {} while (0)

__global__ void __launch_bounds__(768) fused_k(Args a) {
  extern __shared__ __attribute__((aligned(16))) char smem[];
  const unsigned lane = threadIdx.x & 63;
  const int w = __builtin_amdgcn_readfirstlane(threadIdx.x >> 6);
  const int wg = blockIdx.x, xcd = wg & 7, idx = wg >> 3;
  const bool isA = idx < 8;
  const int nu = idx < 24 ? 3 : 2;
  const int bB = nu == 3 ? idx - 8 : (xcd & 1) * 8 + (idx - 24);
  const int slotB = nu == 3 ? xcd : 8 + (xcd >> 1);
  auto headB = [&](int u) { int x = u + bB % 3; x = x >= 3 ? x - 3 : x; return nu == 3 ? 4 * xcd + x : 4 * (xcd ^ u ^ (bB & 1)) + 3; };
  const unsigned L0 = (unsigned)(uintptr_t)(LAS char*)smem;

  if (w < NLD) {
    __builtin_amdgcn_s_setprio(3);
    const unsigned l = w;
    const unsigned e4 = lane >> 4, s16 = lane & 15, r15 = 4 * l + e4;
    const unsigned fT = (r15 & 7) ^ ((r15 & 8) >> 1);
    const unsigned vK = r15 * 256 + ((s16 ^ r15) << 4);
    const unsigned vV = r15 * 256 + ((s16 ^ (fT << 1)) << 4);
    const unsigned vW0 = (lane * 16) ^ (l * 32), vW1 = vW0 ^ 128;
    const unsigned pl = L0 + l * 1024;
    unsigned sl = 0;
    auto kvtile = [&](rsrc_t r, unsigned voff, unsigned so) {
#pragma unroll
      for (int q = 0; q < 8; ++q) dma<2>(r, pl + sl + q * 4096, voff, so + q * 4096);
      sl = nxt(sl);
    };
    auto kvhalf = [&](rsrc_t r, unsigned voff, unsigned so) {
#pragma unroll
      for (int q = 0; q < 4; ++q) dma<2>(r, pl + sl + q * 4096, voff, so + q * 4096);
      sl = nxth(sl);
    };
    auto whalf = [&](rsrc_t r, unsigned so, unsigned rs) {
#pragma unroll
      for (int q = 0; q < 4; ++q) dma<0>(r, pl + sl + q * 4096, ((q ^ (q >> 1)) & 1) ? vW1 : vW0, so + q * 4 * rs);
      sl = nxth(sl);
    };
    auto wtile = [&](rsrc_t r, unsigned so, unsigned rs) { whalf(r, so, rs); whalf(r, so + 16 * rs, rs); };
    if (isA) {
      const rsrc_t rx = mkrsrc(a.x[0], NB * SQ * DM * 2), rk = mkrsrc(a.k[0], NB * 8 * SK * DH * 2), rv = mkrsrc(a.v[0], NB * 8 * SK * DH * 2);
      const rsrc_t rq = mkrsrc(a.wq[0], DM * NH * DH * 2), ro = mkrsrc(a.wo[0], DM * NH * DH * 2);
      const unsigned vX = (lane ^ l) * 16;
      const unsigned xo = (unsigned)idx * (2 * SQ * DM * 2) + l * 1024;
      const unsigned qo = xcd * 1024 + l * 8192;
#pragma unroll
      for (int q = 0; q < 16; ++q) dma<0>(rx, pl + RES_OFF + q * 4096, vX ^ ((q & 3) * 64), xo + q * 4096);
      wtile(rq, qo + ((2 * idx) & 15) * (32 * 8192), 8192);
      wtile(rq, qo + ((2 * idx + 1) & 15) * (32 * 8192), 8192);
#pragma unroll 1
      for (int s = 2; s < 16; ++s) { waitb<8>(); wtile(rq, qo + ((2 * idx + s) & 15) * (32 * 8192), 8192); }
      const unsigned kvo = (unsigned)(2 * idx * 8 + xcd) * (SK * DH * 2);
      waitb<8>(); kvtile(rk, vK, kvo);
      waitb<8>(); kvtile(rv, vV, kvo);
      lbar();
      kvtile(rk, vK, kvo + DT);
      waitb<20>();
      waitb<16>();
#pragma unroll 1
      for (int bk = 1; bk < 8; ++bk) {
        const unsigned so = kvo + (bk >> 2) * (8 * SK * DH * 2) + (bk & 3) * DT;
        waitb<12>(); kvhalf(rv, vV, so);
        waitb<12>(); kvhalf(rv, vV, so + DT / 2);
        if (bk == 7) break;
        const unsigned sn = kvo + ((bk + 1) >> 2) * (8 * SK * DH * 2) + ((bk + 1) & 3) * DT;
        waitb<12>(); kvhalf(rk, vK, sn);
        waitb<12>(); kvhalf(rk, vK, sn + DT / 2);
      }
      const unsigned oo = xcd * (512 * 1024) + l * 1024;
      const unsigned o0 = oo + ((2 * idx) & 15) * DT, o1 = oo + ((2 * idx + 1) & 15) * DT, o2 = oo + ((2 * idx + 2) & 15) * DT;
      waitb<12>(); whalf(ro, o0, 1024);
      waitb<12>(); whalf(ro, o0 + DT / 2, 1024);
      waitb<12>(); whalf(ro, o1, 1024);
      waitb<12>(); whalf(ro, o1 + DT / 2, 1024);
      waitb<8>(); wtile(ro, o2, 1024);
#pragma unroll 1
      for (int s = 3; s < 16; ++s) { waitb<8>(); wtile(ro, oo + ((2 * idx + s) & 15) * DT, 1024); }
      waitb<8>();
      waitb<0>();
    } else {
      const rsrc_t rx = mkrsrc(a.x[1], NB * SQ * DM * 2), rk = mkrsrc(a.k[1], NB * NH * SK * DH * 2), rv = mkrsrc(a.v[1], NB * NH * SK * DH * 2);
      const rsrc_t rq = mkrsrc(a.wq[1], DM * NH * DH * 2), ro = mkrsrc(a.wo[1], DM * NH * DH * 2);
      const unsigned vWq = r15 * 8192 + ((s16 ^ (fT << 1)) << 4);
      const unsigned vX = (lane ^ l) * 16;
      auto qtile = [&](int h, int i) {
        const unsigned so = ((i + bB) & 3) * (128 * 8192) + h * 256;
#pragma unroll
        for (int q = 0; q < 8; ++q) dma<0>(rq, pl + sl + q * 4096, vWq, so + q * (16 * 8192));
        sl = nxt(sl);
      };
      auto otile = [&](int h, int i) { wtile(ro, h * (128 * 1024) + l * 1024 + ((i + bB) & 3) * DT, 1024); };
#define STEPB() do { waitb<16>(); lbar(); } while (0)
      int h = headB(0);
#pragma unroll
      for (int q = 0; q < 8; ++q) dma<0>(rx, pl + X_OFF + q * 4096, vX ^ ((q & 3) * 64), (unsigned)bB * (SQ * DM * 2) + l * 1024 + q * 4096);
      qtile(h, 0); qtile(h, 1); qtile(h, 2);
      unsigned kvo = (unsigned)(bB * NH + h) * (SK * DH * 2);
      STEPB(); qtile(h, 3);
      STEPB(); kvtile(rk, vK, kvo);
      STEPB(); kvtile(rk, vK, kvo + DT);
      STEPB(); kvtile(rk, vK, kvo + 2 * DT);
#pragma unroll 1
      for (int u = 0;; ++u) {
        STEPB(); kvtile(rk, vK, kvo + 3 * DT);
        STEPB(); kvtile(rv, vV, kvo);
        STEPB(); kvtile(rv, vV, kvo + DT);
        STEPB(); lbar(); kvtile(rv, vV, kvo + 2 * DT);
        if (u + 1 == nu) break;
        const int hn = headB(u + 1);
        STEPB(); qtile(hn, 0);
        STEPB(); qtile(hn, 1);
        STEPB(); qtile(hn, 2);
        STEPB(); qtile(hn, 3);
        STEPB(); otile(h, 0);
        STEPB(); otile(h, 1);
        STEPB(); otile(h, 2);
        STEPB(); otile(h, 3);
        STEPB(); kvtile(rv, vV, kvo + 3 * DT);
        h = hn;
        kvo = (unsigned)(bB * NH + h) * (SK * DH * 2);
        STEPB(); kvtile(rk, vK, kvo);
        STEPB(); kvtile(rk, vK, kvo + DT);
        STEPB(); lbar(); kvtile(rk, vK, kvo + 2 * DT);
      }
      STEPB(); otile(h, 0);
      STEPB(); otile(h, 1);
      STEPB(); otile(h, 2);
      STEPB(); otile(h, 3);
      STEPB(); kvtile(rv, vV, kvo + 3 * DT);
      waitb<16>(); lbar();
      waitb<8>(); lbar();
      waitb<0>();
    }
    return;
  }

  const int cw = w - NLD;
  STAMP(0);
  const u64 ep = __hip_atomic_load((const GAS u64*)a.ws, RLX);
  const u64 tag = (((u64)(unsigned)__builtin_amdgcn_readfirstlane((unsigned)(ep >> 32)) << 32) | (unsigned)__builtin_amdgcn_readfirstlane((unsigned)ep)) + 0x9E3779B97F4A7C15ull;
  const rsrc_t rt = mkrsrc(a.ws + WS_TILES, WS_TILES_BYTES);
  unsigned sl = 0;
  auto AK = [&]() { const unsigned l = fresh(lane), c = l & 15, g = l >> 4; return c * 256 + ((g ^ c) << 4); };
  auto AV = [&]() { const unsigned l = fresh(lane), g = l >> 4, qq = (l >> 2) & 3;
                    return (4 * g + qq) * 256 + (((4 * (g & 1) + qq) ^ (4 * (g >> 1))) << 5) + 8 * (l & 3); };
  auto AW = [&]() { const unsigned l = fresh(lane), g = l >> 4, qq = (l >> 2) & 3;
                    return (8 * g + qq) * 1024 + ((((unsigned)cw * 4) ^ (qq ^ (4 * (g & 1)))) << 5) + 8 * (l & 3); };
  auto AF = [&]() { const unsigned l = fresh(lane); return L0 + RING + ((l & 15) + 16 * (l >> 5)) * 16 + 8 * ((l >> 4) & 1); };
  auto AL = [&]() { return L0 + RING + fresh(lane) * 16; };

  if (isA) {
    f32x4 acc[4][4];
    auto gemm = [&](auto xaddr, bool endbar) {
#pragma unroll
      for (int i = 0; i < 4; ++i)
#pragma unroll
        for (int j = 0; j < 4; ++j) acc[i][j] = Z4;
      u32x4 xf[2][4], wf[2][4];
      const unsigned aW = AW();
      auto step = [&](int s, bool domm, bool dord, int cur) {
        const int nx = cur ^ 1;
        const unsigned b = L0 + sl + aW, ax = xaddr((2 * idx + s) & 15);
#pragma unroll
        for (int i = 0; i < 8; ++i) {
          if (dord) {
            if (i < 4) wf[nx][i] = ldtr(b ^ (i << 5), (b ^ (i << 5) ^ 128) + 4096);
            else if (i < 6) { xf[nx][2 * i - 8] = ld128(ax + (2 * i - 8) * 16384); xf[nx][2 * i - 7] = ld128(ax + (2 * i - 7) * 16384); }
          }
          if (domm) {
            acc[i >> 1][2 * (i & 1)] = mfma(wf[cur][i >> 1], xf[cur][2 * (i & 1)], acc[i >> 1][2 * (i & 1)]);
            acc[i >> 1][2 * (i & 1) + 1] = mfma(wf[cur][i >> 1], xf[cur][2 * (i & 1) + 1], acc[i >> 1][2 * (i & 1) + 1]);
          }
          SB();
        }
        if (dord) sl = nxt(sl);
      };
      cbar(); step(0, false, true, 1);
#pragma unroll 1
      for (int s = 1; s < 15; s += 2) {
        cbar(); step(s, true, true, 0);
        cbar(); step(s + 1, true, true, 1);
      }
      cbar(); step(15, true, true, 0);
      if (endbar) cbar();
      step(0, true, false, 1);
    };
    {
      const unsigned l = fresh(lane), c = l & 15, g = l >> 4;
      gemm([&](unsigned ks) { return L0 + RES_OFF + c * 1024 + (((4 * ks + g) ^ c) << 4); }, true);
    }
    STAMP(1);
    {
      const unsigned aF = AF() + ((cw >> 1) * 8 + 2 * (cw & 1)) * 1024;
#pragma unroll
      for (int nb = 0; nb < 4; ++nb)
#pragma unroll
        for (int bq = 0; bq < 4; ++bq)
          st64(aF + (bq >> 1) * 32768 + ((bq & 1) * 4 + (nb >> 1)) * 1024 + (nb & 1) * 512, cvtpk(acc[nb][bq][0], acc[nb][bq][1]), cvtpk(acc[nb][bq][2], acc[nb][bq][3]));
    }
    const unsigned aK = AK(), aV = AV();
    constexpr int PD = 6;
    u32x4 fr[8];
    auto rdK = [&](unsigned b, int i) { fr[i & 7] = ld128((b ^ ((i & 3) << 6)) + (i >> 2) * 4096); };
    auto rdV = [&](unsigned b, int i) { const unsigned ad = (b ^ ((i & 7) << 5)) + (i >> 3) * 8192; fr[i & 7] = ldtr(ad, ad + 4096); };
    cbar();
    {
      const unsigned b = L0 + sl + aK;
#pragma unroll
      for (int i = 0; i < PD; ++i) rdK(b, i);
    }
#pragma unroll 1
    for (int bt = 0; bt < 2; ++bt) {
      u32x4 qf[4];
      {
        const unsigned aq = AL() + bt * 32768 + cw * 4096;
#pragma unroll
        for (int kk = 0; kk < 4; ++kk) qf[kk] = ld128(aq + kk * 1024);
      }
      f32x4 o[8];
#pragma unroll
      for (int i = 0; i < 8; ++i) o[i] = Z4;
      float m = -INFINITY, ls = 0.f;
#pragma unroll 1
      for (int bk = 0; bk < 4; ++bk) {
        f32x4 s[8];
        u32x4 pf[4];
#pragma unroll
        for (int i = 0; i < 8; ++i) s[i] = Z4;
#pragma unroll
        for (int t = 0; t < 4; ++t) {
          const unsigned bc = L0 + sl + (t < 2 ? aK : aV);
          sl = nxth(sl);
          const unsigned bn = L0 + sl + ((t == 0 || t == 3) ? aK : aV);
          const bool last = t == 3 && bt == 1 && bk == 3;
#pragma unroll
          for (int i = 0; i < 16; ++i) {
            if (i == 16 - PD && !last) { SB(); lbar(); SB(); }
            const u32x4 f = fr[i & 7];
            if (t < 2) s[t * 4 + (i >> 2)] = mfma(f, qf[i & 3], s[t * 4 + (i >> 2)]);
            else o[i & 7] = mfma(f, pf[(t - 2) * 2 + (i >> 3)], o[i & 7]);
            if (i + PD < 16) { if (t < 2) rdK(bc, i + PD); else rdV(bc, i + PD); }
            else { if (t == 0 || t == 3) rdK(bn, i + PD - 16); else rdV(bn, i + PD - 16); }
            SB();
          }
          if (t == 1) {
            float mx = s[0][0];
#pragma unroll
            for (int kb = 0; kb < 8; ++kb)
#pragma unroll
              for (int r = 0; r < 4; ++r) mx = fmaxf(mx, s[kb][r]);
            mx = xmax4(mx);
            const float mn = fmaxf(m, mx * SL2E);
            const float alpha = __builtin_amdgcn_exp2f(m - mn);
            m = mn;
            float ps = 0.f;
#pragma unroll
            for (int kb = 0; kb < 8; ++kb) {
#pragma unroll
              for (int r = 0; r < 4; ++r) { s[kb][r] = __builtin_amdgcn_exp2f(fmaf(s[kb][r], SL2E, -mn)); ps += s[kb][r]; }
              pf[kb >> 1][2 * (kb & 1)] = cvtpk(s[kb][0], s[kb][1]);
              pf[kb >> 1][2 * (kb & 1) + 1] = cvtpk(s[kb][2], s[kb][3]);
            }
            ls = ls * alpha + ps;
#pragma unroll
            for (int i = 0; i < 8; ++i) o[i] *= alpha;
            SB();
          }
        }
      }
      const float inv = 1.f / xsum4(ls);
      const unsigned aF = AF() + ((bt * 2 + (cw & 1)) * 16 + (cw >> 1) * 4) * 1024;
#pragma unroll
      for (int db = 0; db < 8; ++db) st64(aF + db * 512, cvtpk(o[db][0] * inv, o[db][1] * inv), cvtpk(o[db][2] * inv, o[db][3] * inv));
      STAMP(2 + bt);
    }
    {
      const unsigned al = AL();
      gemm([&](unsigned ks) { return al + ks * 1024; }, false);
    }
    STAMP(4);
#pragma unroll
    for (int bq = 0; bq < 4; ++bq)
#pragma unroll
      for (int np = 0; np < 2; ++np) {
        const u32x4 v = {cvtpk(acc[2 * np][bq][0], acc[2 * np][bq][1]), cvtpk(acc[2 * np][bq][2], acc[2 * np][bq][3]),
                         cvtpk(acc[2 * np + 1][bq][0], acc[2 * np + 1][bq][1]), cvtpk(acc[2 * np + 1][bq][2], acc[2 * np + 1][bq][3])};
        __builtin_amdgcn_raw_buffer_store_b128(v, rt, lane * 16, WS_TILES_A + ((2 * idx + (bq >> 1)) * 8 + xcd) * TILE_B + ((cw * 2 + np) * 2 + (bq & 1)) * 1024, 16);
      }
    cbar_vm();
    if (cw == 0 && lane < 2) __hip_atomic_store((GAS u64*)(a.ws + WS_FLAGA + ((2 * idx + lane) * 8 + xcd) * 8), tag, RLX);
  } else {
    f32x4 oa[4][2];
#pragma unroll
    for (int nb = 0; nb < 4; ++nb) { oa[nb][0] = Z4; oa[nb][1] = Z4; }
    auto qproj = [&](bool first) {
      f32x4 acc[2] = {Z4, Z4};
      const unsigned l = fresh(lane), c = l & 15, g = l >> 4, qq = c >> 2;
      const unsigned aq = (8 * g + qq) * 256 + (((unsigned)cw ^ (qq ^ (4 * (g & 1)))) << 5) + 8 * (l & 3);
      const unsigned ax0 = L0 + X_OFF + c * 1024 + ((g ^ c) << 4);
      u32x4 xf[2][8];
      auto rdx = [&](int st) {
        const unsigned ax = ax0 + ((st + bB) & 3) * 256;
#pragma unroll
        for (int i = 0; i < 8; ++i) xf[st & 1][i] = ld128((ax ^ ((i >> 1) << 6)) + (i & 1) * 16384);
      };
      if (!first) rdx(0);
#pragma unroll
      for (int st = 0; st < 4; ++st) {
        cbar();
        const unsigned b = L0 + sl + aq;
        u32x4 wf[4];
#pragma unroll
        for (int ks = 0; ks < 4; ++ks) wf[ks] = ldtr(b + ks * 8192, (b ^ 128) + 1024 + ks * 8192);
        if (first && st == 0) rdx(0);
        cbar();
        if (st < 3) rdx(st + 1);
        SB();
#pragma unroll
        for (int i = 0; i < 8; ++i) acc[i & 1] = mfma(wf[i >> 1], xf[st & 1][i], acc[i & 1]);
        sl = nxt(sl);
      }
#pragma unroll
      for (int qb = 0; qb < 2; ++qb) st64(AF() + cw * 512 + (Q_OFF - RING) + qb * 4096, cvtpk(acc[qb][0], acc[qb][1]), cvtpk(acc[qb][2], acc[qb][3]));
    };
    qproj(true);
    STAMP(8);
#pragma unroll 1
    for (int u = 0; u < nu; ++u) {
      f32x4 s[4][2];
      {
        u32x4 qf[2][4];
        const unsigned aK = AK() + cw * 4096;
#pragma unroll
        for (int dl = 0; dl < 4; ++dl) {
          cbar();
          if (u == 1 && (dl == 0 || dl == 3)) STAMP(9 + dl);
          const unsigned b = L0 + sl + aK;
          u32x4 kf[4];
#pragma unroll
          for (int kk = 0; kk < 4; ++kk) kf[kk] = ld128(b ^ (kk << 6));
          if (dl == 0) {
            const unsigned al = AL();
#pragma unroll
            for (int qb = 0; qb < 2; ++qb)
#pragma unroll
              for (int kk = 0; kk < 4; ++kk) qf[qb][kk] = ld128(al + (Q_OFF - RING) + (qb * 4 + kk) * 1024);
          }
          cbar();
          s[dl][0] = Z4; s[dl][1] = Z4;
#pragma unroll
          for (int kk = 0; kk < 4; ++kk) { s[dl][0] = mfma(kf[kk], qf[0][kk], s[dl][0]); s[dl][1] = mfma(kf[kk], qf[1][kk], s[dl][1]); }
          sl = nxt(sl);
        }
      }
      if (u == 0) STAMP(1);
      const unsigned aS = L0 + ST_OFF + (fresh(lane) & 15) * 32;
#pragma unroll
      for (int qb = 0; qb < 2; ++qb) {
        float mx = s[0][qb][0];
#pragma unroll
        for (int dl = 0; dl < 4; ++dl)
#pragma unroll
          for (int r = 0; r < 4; ++r) mx = fmaxf(mx, s[dl][qb][r]);
        mx = xmax4(mx);
        if (lane < 16) *(LAS float*)(uintptr_t)(aS + qb * 512 + cw * 4) = mx;
      }
      cbar();
      u32x2 pk[2][2];
      const unsigned oP = (P_OFF - RING) + (cw >> 1) * 2048 + (cw & 1) * 8;
      {
        const unsigned aP = AL() + oP;
#pragma unroll
        for (int qb = 0; qb < 2; ++qb) {
          const f32x4 m0 = ldf4(aS + qb * 512), m1 = ldf4(aS + qb * 512 + 16);
          const float mn = SL2E * fmaxf(fmaxf(fmaxf(m0[0], m0[1]), fmaxf(m0[2], m0[3])), fmaxf(fmaxf(m1[0], m1[1]), fmaxf(m1[2], m1[3])));
          float ps = 0.f;
#pragma unroll
          for (int dl = 0; dl < 4; ++dl) {
#pragma unroll
            for (int r = 0; r < 4; ++r) { s[dl][qb][r] = __builtin_amdgcn_exp2f(fmaf(s[dl][qb][r], SL2E, -mn)); ps += s[dl][qb][r]; }
            const unsigned lo = cvtpk(s[dl][qb][0], s[dl][qb][1]), hi = cvtpk(s[dl][qb][2], s[dl][qb][3]);
            if (dl < 2) st64(aP + dl * 8192 + qb * 1024, lo, hi); else pk[dl - 2][qb] = u32x2{lo, hi};
          }
          ps = xsum4(ps);
          if (lane < 16) *(LAS float*)(uintptr_t)(aS + 1024 + qb * 512 + cw * 4) = ps;
        }
      }
      f32x4 o[2] = {Z4, Z4};
      auto vstep = [&](int dl, bool pre) {
        const unsigned ap = AL() + (P_OFF - RING) + (dl & 1) * 8192;
        u32x4 pf[4][2], vf[4];
        if (pre) {
#pragma unroll
          for (int i = 0; i < 8; ++i) pf[i >> 1][i & 1] = ld128(ap + i * 1024);
        }
        cbar();
        if (u == 1 && dl >= 2) STAMP(dl == 2 ? 16 : 22);
        const unsigned b = (L0 + sl + AV()) ^ ((unsigned)cw << 5);
#pragma unroll
        for (int ks = 0; ks < 4; ++ks) vf[ks] = ldtr(b + ks * 8192, b + ks * 8192 + 4096);
        if (!pre) {
#pragma unroll
          for (int i = 0; i < 8; ++i) pf[i >> 1][i & 1] = ld128(ap + i * 1024);
        }
        cbar();
#pragma unroll
        for (int ks = 0; ks < 4; ++ks)
#pragma unroll
          for (int qb = 0; qb < 2; ++qb) o[qb] = mfma(vf[ks], pf[ks][qb], o[qb]);
        sl = nxt(sl);
      };
      vstep(0, false);
      vstep(1, true);
      {
        const unsigned aP = AL() + oP;
#pragma unroll
        for (int i = 0; i < 2; ++i)
#pragma unroll
          for (int qb = 0; qb < 2; ++qb) st64(aP + i * 8192 + qb * 1024, pk[i][qb][0], pk[i][qb][1]);
      }
      float inv[2];
#pragma unroll
      for (int qb = 0; qb < 2; ++qb) {
        const f32x4 l0 = ldf4(aS + 1024 + qb * 512), l1 = ldf4(aS + 1024 + qb * 512 + 16);
        inv[qb] = 1.f / (((l0[0] + l0[1]) + (l0[2] + l0[3])) + ((l1[0] + l1[1]) + (l1[2] + l1[3])));
      }
      vstep(2, false);
      if (u + 1 < nu) qproj(false);
      if (u == 1) STAMP(20);
      u32x4 wo[4][4];
#pragma unroll
      for (int kk = 0; kk < 4; ++kk) {
        cbar();
        const unsigned b = L0 + sl + AW();
#pragma unroll
        for (int nb = 0; nb < 4; ++nb) wo[kk][nb] = ldtr(b ^ (nb << 5), (b ^ (nb << 5) ^ 128) + 4096);
        cbar();
        sl = nxt(sl);
      }
      vstep(3, true);
#pragma unroll
      for (int qb = 0; qb < 2; ++qb) {
        const f32x4 ov = o[qb] * inv[qb];
        st64(AF() + cw * 512 + (P_OFF - RING) + qb * 4096, cvtpk(ov[0], ov[1]), cvtpk(ov[2], ov[3]));
      }
      cbar();
      {
        u32x4 of[2][4];
        const unsigned al = AL() + (P_OFF - RING);
#pragma unroll
        for (int kk = 0; kk < 4; ++kk)
#pragma unroll
          for (int qb = 0; qb < 2; ++qb) of[qb][kk] = ld128(al + ((kk + bB) & 3) * 1024 + qb * 4096);
        SB();
#pragma unroll
        for (int kk = 0; kk < 4; ++kk)
#pragma unroll
          for (int qb = 0; qb < 2; ++qb)
#pragma unroll
            for (int nb = 0; nb < 4; ++nb) oa[nb][qb] = mfma(wo[kk][nb], of[qb][kk], oa[nb][qb]);
      }
      STAMP(2 + u);
    }
#pragma unroll
    for (int qb = 0; qb < 2; ++qb)
#pragma unroll
      for (int np = 0; np < 2; ++np) {
        const u32x4 v = {cvtpk(oa[2 * np][qb][0], oa[2 * np][qb][1]), cvtpk(oa[2 * np][qb][2], oa[2 * np][qb][3]),
                         cvtpk(oa[2 * np + 1][qb][0], oa[2 * np + 1][qb][1]), cvtpk(oa[2 * np + 1][qb][2], oa[2 * np + 1][qb][3])};
        __builtin_amdgcn_raw_buffer_store_b128(v, rt, lane * 16, (bB * 12 + slotB) * TILE_B + ((cw * 2 + np) * 2 + qb) * 1024, 16);
      }
    cbar_vm();
    if (cw == 0 && lane == 0) __hip_atomic_store((GAS u64*)(a.ws + WS_FLAGB + (bB * 16 + slotB) * 8), tag, RLX);
  }
  STAMP(5);

  {
    const unsigned c = lane & 15, g = lane >> 4;
    const int rb = wg >> 4, pc = (wg & 15) * 2 + ((cw >> 1) & 1), tg = cw & 1;
    const bool wB = cw < 4;
    const unsigned nt = wB ? 6 : 4;
    const unsigned fl = lane < nt ? lane : 0;
    const GAS u64* fp = (const GAS u64*)(a.ws + (wB ? WS_FLAGB + (rb * 16 + tg * 6 + fl) * 8 : WS_FLAGA + (rb * 8 + tg * 4 + fl) * 8));
#define NSPIN (1 << 16)
    for (int spin = 0; spin < NSPIN; ++spin) {
      if (__all(__hip_atomic_load(fp, RLX) == tag)) break;
      __builtin_amdgcn_s_sleep(4);
    }
    __builtin_amdgcn_fence(__ATOMIC_ACQUIRE, "wavefront");
    STAMP(6);
    const unsigned so = (wB ? (rb * 12 + tg * 6) * TILE_B : WS_TILES_A + (rb * 8 + tg * 4) * TILE_B) + pc * 1024;
    u32x4 t[6];
#pragma unroll
    for (int i = 0; i < 6; ++i) t[i] = __builtin_amdgcn_raw_buffer_load_b128(rt, (i < 4 || wB) ? lane * 16 : 0x7fffff00u, so + i * TILE_B, 16);
    const f32x4 s0 = ((bflo(t[0]) + bflo(t[1])) + (bflo(t[2]) + bflo(t[3]))) + (bflo(t[4]) + bflo(t[5]));
    const f32x4 s1 = ((bfhi(t[0]) + bfhi(t[1])) + (bfhi(t[2]) + bfhi(t[3]))) + (bfhi(t[4]) + bfhi(t[5]));
    *(LAS f32x4*)(uintptr_t)(L0 + cw * 1024 + lane * 16) = tg ? s0 : s1;
    cbar();
    const f32x4 oth = ldf4(L0 + (cw ^ 1) * 1024 + lane * 16);
    const f32x4 r = tg ? oth + s1 : s0 + oth;
    *(GAS u32x2*)(a.out + ((size_t)(rb * SQ + (pc & 1) * 16 + c) * 1024 + (wB ? 512 : 0) + 32 * (pc >> 1) + 16 * tg + 4 * g) * 2) = u32x2{cvtpk(r[0], r[1]), cvtpk(r[2], r[3])};
    if (wg == 255 && cw == 1) {
      for (int spin = 0; spin < NSPIN; ++spin) {
        bool ok = true;
#pragma unroll
        for (int i = 0; i < 4; ++i) ok &= (lane & 15) >= 12 || __hip_atomic_load((const GAS u64*)(a.ws + WS_FLAGB + (i * 64 + lane) * 8), RLX) == tag;
#pragma unroll
        for (int i = 0; i < 2; ++i) ok &= __hip_atomic_load((const GAS u64*)(a.ws + WS_FLAGA + (i * 64 + lane) * 8), RLX) == tag;
        if (__all(ok)) break;
        __builtin_amdgcn_s_sleep(2);
      }
      if (lane == 0) __hip_atomic_store((GAS u64*)a.ws, tag, RLX);
    }
  }
  STAMP(7);
}

extern "C" void kernel_launch(void* const* d_in, const int* in_sizes, int n_in, void* d_out, int out_size, void* d_ws, size_t ws_size,
                              hipStream_t stream) {
  Args a;
  for (int br = 0; br < 2; ++br) {
    a.x[br] = d_in[5 * br + 0];
    a.k[br] = d_in[5 * br + 1];
    a.v[br] = d_in[5 * br + 2];
    a.wq[br] = d_in[5 * br + 3];
    a.wo[br] = d_in[5 * br + 4];
  }
  a.ws = (char*)d_ws;
  a.out = (char*)d_out;
  (void)hipFuncSetAttribute((const void*)fused_k, hipFuncAttributeMaxDynamicSharedMemorySize, LDS_BYTES);
  fused_k<<<256, 768, LDS_BYTES, stream>>>(a);
}
```

```cpp
#define STAMPS
#include <hip/hip_runtime.h>
#include <stdint.h>

#define LAS __attribute__((address_space(3)))
#define GAS __attribute__((address_space(1)))
typedef unsigned u32x4 __attribute__((ext_vector_type(4)));
typedef unsigned u32x2 __attribute__((ext_vector_type(2)));
typedef float f32x4 __attribute__((ext_vector_type(4)));
typedef short bf16x8 __attribute__((ext_vector_type(8)));
typedef short s16x4 __attribute__((ext_vector_type(4)));
typedef __amdgpu_buffer_rsrc_t rsrc_t;
typedef unsigned long long u64;

constexpr int NB = 16, SQ = 32, DM = 512, NH = 32, SK = 512, DH = 128;
constexpr float SL2E = 0.088388347648318447f * 1.4426950408889634f;
constexpr unsigned DT = 32768;
constexpr unsigned RING = 3 * DT;
constexpr unsigned X_OFF = RING, P_OFF = RING + 32768, Q_OFF = P_OFF + 16384, ST_OFF = Q_OFF + 8192;
constexpr unsigned RES_OFF = RING;
constexpr unsigned LDS_BYTES = 160 * 1024;
constexpr int NLD = 4;
constexpr unsigned WS_FLAGB = 4096, WS_FLAGA = 8192 + 4096, WS_TILES = 1u << 20;
constexpr unsigned TILE_B = 32768;
constexpr unsigned WS_TILES_A = 192 * TILE_B;
constexpr unsigned WS_TILES_BYTES = 320 * TILE_B;

struct Args {
  const void* x[2]; const void* k[2]; const void* v[2]; const void* wq[2]; const void* wo[2];
  char* ws; char* out;
  u64* stamps;
};

__device__ __forceinline__ rsrc_t mkrsrc(const void* p, unsigned bytes) { return __builtin_amdgcn_make_buffer_rsrc((void*)p, 0, bytes, 0x00020000); }
template <int AUX> __device__ __forceinline__ void dma(rsrc_t r, unsigned lds, unsigned voff, unsigned soff) {
  __builtin_amdgcn_raw_ptr_buffer_load_lds(r, (LAS void*)(uintptr_t)lds, 16, voff, soff, 0, AUX);
}
template <int N> __device__ __forceinline__ void waitb() { asm volatile("s_waitcnt vmcnt(%0)\n\ts_barrier" :: "i"(N) : "memory"); }
__device__ __forceinline__ void lbar() { asm volatile("s_barrier" ::: "memory"); }
__device__ __forceinline__ void cbar() { __builtin_amdgcn_sched_barrier(0); asm volatile("s_waitcnt lgkmcnt(0)\n\ts_barrier" ::: "memory"); __builtin_amdgcn_sched_barrier(0); }
__device__ __forceinline__ void cbar_vm() { __builtin_amdgcn_sched_barrier(0); asm volatile("s_waitcnt vmcnt(0) lgkmcnt(0)\n\ts_barrier" ::: "memory"); __builtin_amdgcn_sched_barrier(0); }

__device__ __forceinline__ f32x4 mfma(u32x4 a, u32x4 b, f32x4 c) {
  return __builtin_amdgcn_mfma_f32_16x16x32_bf16(__builtin_bit_cast(bf16x8, a), __builtin_bit_cast(bf16x8, b), c, 0, 0, 0);
}
typedef float f32x2 __attribute__((ext_vector_type(2)));
typedef __bf16 bf16x2 __attribute__((ext_vector_type(2)));
__device__ __forceinline__ unsigned cvtpk(float lo, float hi) {
  return __builtin_bit_cast(unsigned, __builtin_convertvector(f32x2{lo, hi}, bf16x2));
}
__device__ __forceinline__ u32x4 ld128(unsigned a) {
  return *(const LAS u32x4*)(uintptr_t)a; }
__device__ __forceinline__ f32x4 ldf4(unsigned a) { return *(const LAS f32x4*)(uintptr_t)a; }
__device__ __forceinline__ void st64(unsigned a, unsigned lo, unsigned hi) { *(LAS u32x2*)(uintptr_t)a = u32x2{lo, hi}; }
__device__ __forceinline__ u32x4 ldtr(unsigned a0, unsigned a1) {
  const u32x2 lo = __builtin_bit_cast(u32x2, __builtin_amdgcn_ds_read_tr16_b64_v4i16((LAS s16x4*)(uintptr_t)a0));
  const u32x2 hi = __builtin_bit_cast(u32x2, __builtin_amdgcn_ds_read_tr16_b64_v4i16((LAS s16x4*)(uintptr_t)a1));
  return u32x4{lo[0], lo[1], hi[0], hi[1]};
}
__device__ __forceinline__ float xmax4(float x) {
  auto s = __builtin_amdgcn_permlane16_swap(__float_as_uint(x), __float_as_uint(x), false, false);
  x = fmaxf(__uint_as_float(s[0]), __uint_as_float(s[1]));
  auto t = __builtin_amdgcn_permlane32_swap(__float_as_uint(x), __float_as_uint(x), false, false);
  return fmaxf(__uint_as_float(t[0]), __uint_as_float(t[1]));
}
__device__ __forceinline__ float xsum4(float x) {
  auto s = __builtin_amdgcn_permlane16_swap(__float_as_uint(x), __float_as_uint(x), false, false);
  x = __uint_as_float(s[0]) + __uint_as_float(s[1]);
  auto t = __builtin_amdgcn_permlane32_swap(__float_as_uint(x), __float_as_uint(x), false, false);
  return __uint_as_float(t[0]) + __uint_as_float(t[1]);
}
__device__ __forceinline__ unsigned nxt(unsigned s) { s += DT; return s == RING ? 0u : s; }
__device__ __forceinline__ unsigned nxth(unsigned s) { s += DT / 2; return s == RING ? 0u : s; }
#define ABL_OFF 0
#define SB() __builtin_amdgcn_sched_barrier(0)
__device__ __forceinline__ unsigned fresh(unsigned x) { asm volatile("" : "+v"(x)); return x; }
#define RLX __ATOMIC_RELAXED, __HIP_MEMORY_SCOPE_AGENT
#define KFIRST_MASK 0x2a
__device__ __forceinline__ f32x4 bflo(u32x4 v) { return f32x4{__uint_as_float(v[0] << 16), __uint_as_float(v[0] & 0xffff0000u), __uint_as_float(v[1] << 16), __uint_as_float(v[1] & 0xffff0000u)}; }
__device__ __forceinline__ f32x4 bfhi(u32x4 v) { return f32x4{__uint_as_float(v[2] << 16), __uint_as_float(v[2] & 0xffff0000u), __uint_as_float(v[3] << 16), __uint_as_float(v[3] & 0xffff0000u)}; }

#define Z4 f32x4{0.f, 0.f, 0.f, 0.f}
#define STAMP(i) do { unsigned t_ = (unsigned)__builtin_amdgcn_s_memrealtime(); _Pragma("unroll") for (int k_ = 0; k_ < 30; ++k_) if (k_ == (i)) stv[k_] = t_; } while (0)

__global__ void __launch_bounds__(768) fused_k(Args a) {
  extern __shared__ __attribute__((aligned(16))) char smem[];
  const unsigned lane = threadIdx.x & 63;
  const int w = __builtin_amdgcn_readfirstlane(threadIdx.x >> 6);
  const int wg = blockIdx.x, xcd = wg & 7, idx = wg >> 3;
  const bool isA = idx < 8;
  const int nu = idx < 24 ? 3 : 2;
  const int bB = nu == 3 ? idx - 8 : (xcd & 1) * 8 + (idx - 24);
  const int slotB = nu == 3 ? xcd : 8 + (xcd >> 1);
  auto headB = [&](int u) { int x = u + bB % 3; x = x >= 3 ? x - 3 : x; return nu == 3 ? 4 * xcd + x : 4 * (xcd ^ u ^ (bB & 1)) + 3; };
  const unsigned L0 = (unsigned)(uintptr_t)(LAS char*)smem;

  if (w < NLD) {
    __builtin_amdgcn_s_setprio(3);
    const unsigned l = w;
    const unsigned e4 = lane >> 4, s16 = lane & 15, r15 = 4 * l + e4;
    const unsigned fT = (r15 & 7) ^ ((r15 & 8) >> 1);
    const unsigned vK = r15 * 256 + ((s16 ^ r15) << 4);
    const unsigned vV = r15 * 256 + ((s16 ^ (fT << 1)) << 4);
    const unsigned vW0 = (lane * 16) ^ (l * 32), vW1 = vW0 ^ 128;
    const unsigned pl = L0 + l * 1024;
    unsigned sl = 0;
    auto kvtile = [&](rsrc_t r, unsigned voff, unsigned so) {
#pragma unroll
      for (int q = 0; q < 8; ++q) dma<2>(r, pl + sl + q * 4096, voff, so + q * 4096);
      sl = nxt(sl);
    };
    auto kvhalf = [&](rsrc_t r, unsigned voff, unsigned so) {
#pragma unroll
      for (int q = 0; q < 4; ++q) dma<2>(r, pl + sl + q * 4096, voff, so + q * 4096);
      sl = nxth(sl);
    };
    auto whalf = [&](rsrc_t r, unsigned so, unsigned rs) {
#pragma unroll
      for (int q = 0; q < 4; ++q) dma<0>(r, pl + sl + q * 4096, ((q ^ (q >> 1)) & 1) ? vW1 : vW0, so + q * 4 * rs);
      sl = nxth(sl);
    };
    auto wtile = [&](rsrc_t r, unsigned so, unsigned rs) { whalf(r, so, rs); whalf(r, so + 16 * rs, rs); };
    if (isA) {
      const rsrc_t rx = mkrsrc(a.x[0], NB * SQ * DM * 2), rk = mkrsrc(a.k[0], NB * 8 * SK * DH * 2), rv = mkrsrc(a.v[0], NB * 8 * SK * DH * 2);
      const rsrc_t rq = mkrsrc(a.wq[0], DM * NH * DH * 2), ro = mkrsrc(a.wo[0], DM * NH * DH * 2);
      const unsigned vX = (lane ^ l) * 16;
      const unsigned xo = (unsigned)idx * (2 * SQ * DM * 2) + l * 1024;
      const unsigned qo = xcd * 1024 + l * 8192;
#pragma unroll
      for (int q = 0; q < 16; ++q) dma<0>(rx, pl + RES_OFF + q * 4096, vX ^ ((q & 3) * 64), xo + q * 4096);
      wtile(rq, qo + ((2 * idx) & 15) * (32 * 8192), 8192);
      wtile(rq, qo + ((2 * idx + 1) & 15) * (32 * 8192), 8192);
#pragma unroll 1
      for (int s = 2; s < 16; ++s) { waitb<8>(); wtile(rq, qo + ((2 * idx + s) & 15) * (32 * 8192), 8192); }
      const unsigned kvo = (unsigned)(2 * idx * 8 + xcd) * (SK * DH * 2);
      waitb<8>(); kvtile(rk, vK, kvo);
      waitb<8>(); kvtile(rv, vV, kvo);
      lbar();
      kvtile(rk, vK, kvo + DT);
      waitb<20>();
      waitb<16>();
#pragma unroll 1
      for (int bk = 1; bk < 8; ++bk) {
        const unsigned so = kvo + (bk >> 2) * (8 * SK * DH * 2) + (bk & 3) * DT;
        waitb<12>(); kvhalf(rv, vV, so);
        waitb<12>(); kvhalf(rv, vV, so + DT / 2);
        if (bk == 7) break;
        const unsigned sn = kvo + ((bk + 1) >> 2) * (8 * SK * DH * 2) + ((bk + 1) & 3) * DT;
        waitb<12>(); kvhalf(rk, vK, sn);
        waitb<12>(); kvhalf(rk, vK, sn + DT / 2);
      }
      const unsigned oo = xcd * (512 * 1024) + l * 1024;
      const unsigned o0 = oo + ((2 * idx) & 15) * DT, o1 = oo + ((2 * idx + 1) & 15) * DT, o2 = oo + ((2 * idx + 2) & 15) * DT;
      waitb<12>(); whalf(ro, o0, 1024);
      waitb<12>(); whalf(ro, o0 + DT / 2, 1024);
      waitb<12>(); whalf(ro, o1, 1024);
      waitb<12>(); whalf(ro, o1 + DT / 2, 1024);
      waitb<8>(); wtile(ro, o2, 1024);
#pragma unroll 1
      for (int s = 3; s < 16; ++s) { waitb<8>(); wtile(ro, oo + ((2 * idx + s) & 15) * DT, 1024); }
      waitb<8>();
      waitb<0>();
    } else {
      const rsrc_t rx = mkrsrc(a.x[1], NB * SQ * DM * 2), rk = mkrsrc(a.k[1], NB * NH * SK * DH * 2), rv = mkrsrc(a.v[1], NB * NH * SK * DH * 2);
      const rsrc_t rq = mkrsrc(a.wq[1], DM * NH * DH * 2), ro = mkrsrc(a.wo[1], DM * NH * DH * 2);
      const unsigned vWq = r15 * 8192 + ((s16 ^ (fT << 1)) << 4);
      const unsigned vX = (lane ^ l) * 16;
      auto qtile = [&](int h, int i) {
        const unsigned so = ((i + bB) & 3) * (128 * 8192) + h * 256;
#pragma unroll
        for (int q = 0; q < 8; ++q) dma<0>(rq, pl + sl + q * 4096, vWq, so + q * (16 * 8192));
        sl = nxt(sl);
      };
      auto otile = [&](int h, int i) { wtile(ro, h * (128 * 1024) + l * 1024 + ((i + bB) & 3) * DT, 1024); };
#define STEPB() do { waitb<16>(); lbar(); } while (0)
      int h = headB(0);
#pragma unroll
      for (int q = 0; q < 8; ++q) dma<0>(rx, pl + X_OFF + q * 4096, vX ^ ((q & 3) * 64), (unsigned)bB * (SQ * DM * 2) + l * 1024 + q * 4096);
      qtile(h, 0); qtile(h, 1); qtile(h, 2);
      unsigned kvo = (unsigned)(bB * NH + h) * (SK * DH * 2);
      STEPB(); qtile(h, 3);
      STEPB(); kvtile(rk, vK, kvo);
      STEPB(); kvtile(rk, vK, kvo + DT);
      STEPB(); kvtile(rk, vK, kvo + 2 * DT);
#pragma unroll 1
      for (int u = 0;; ++u) {
        STEPB(); kvtile(rk, vK, kvo + 3 * DT);
        STEPB(); kvtile(rv, vV, kvo);
        STEPB(); kvtile(rv, vV, kvo + DT);
        STEPB(); lbar(); kvtile(rv, vV, kvo + 2 * DT);
        if (u + 1 == nu) break;
        const int hn = headB(u + 1);
        STEPB(); qtile(hn, 0);
        STEPB(); qtile(hn, 1);
        STEPB(); qtile(hn, 2);
        STEPB(); qtile(hn, 3);
        STEPB(); otile(h, 0);
        STEPB(); otile(h, 1);
        STEPB(); otile(h, 2);
        STEPB(); otile(h, 3);
        STEPB(); kvtile(rv, vV, kvo + 3 * DT);
        h = hn;
        kvo = (unsigned)(bB * NH + h) * (SK * DH * 2);
        STEPB(); kvtile(rk, vK, kvo);
        STEPB(); kvtile(rk, vK, kvo + DT);
        STEPB(); lbar(); kvtile(rk, vK, kvo + 2 * DT);
      }
      STEPB(); otile(h, 0);
      STEPB(); otile(h, 1);
      STEPB(); otile(h, 2);
      STEPB(); otile(h, 3);
      STEPB(); kvtile(rv, vV, kvo + 3 * DT);
      waitb<16>(); lbar();
      waitb<8>(); lbar();
      waitb<0>();
    }
    return;
  }

  const int cw = w - NLD;
  unsigned stv[30];
  _Pragma("unroll") for (int k_ = 0; k_ < 30; ++k_) stv[k_] = 0;
  STAMP(0);
  const u64 clk0 = __builtin_amdgcn_s_memtime();
  const u64 ep = __hip_atomic_load((const GAS u64*)a.ws, RLX);
  const u64 tag = (((u64)(unsigned)__builtin_amdgcn_readfirstlane((unsigned)(ep >> 32)) << 32) | (unsigned)__builtin_amdgcn_readfirstlane((unsigned)ep)) + 0x9E3779B97F4A7C15ull;
  const rsrc_t rt = mkrsrc(a.ws + WS_TILES, WS_TILES_BYTES);
  unsigned sl = 0;
  auto AK = [&]() { const unsigned l = fresh(lane), c = l & 15, g = l >> 4; return c * 256 + ((g ^ c) << 4); };
  auto AV = [&]() { const unsigned l = fresh(lane), g = l >> 4, qq = (l >> 2) & 3;
                    return (4 * g + qq) * 256 + (((4 * (g & 1) + qq) ^ (4 * (g >> 1))) << 5) + 8 * (l & 3); };
  auto AW = [&]() { const unsigned l = fresh(lane), g = l >> 4, qq = (l >> 2) & 3;
                    return (8 * g + qq) * 1024 + ((((unsigned)cw * 4) ^ (qq ^ (4 * (g & 1)))) << 5) + 8 * (l & 3); };
  auto AF = [&]() { const unsigned l = fresh(lane); return L0 + RING + ((l & 15) + 16 * (l >> 5)) * 16 + 8 * ((l >> 4) & 1); };
  auto AL = [&]() { return L0 + RING + fresh(lane) * 16; };

  if (isA) {
    f32x4 acc[4][4];
    auto gemm = [&](auto xaddr, bool endbar) {
#pragma unroll
      for (int i = 0; i < 4; ++i)
#pragma unroll
        for (int j = 0; j < 4; ++j) acc[i][j] = Z4;
      u32x4 xf[2][4], wf[2][4];
      const unsigned aW = AW();
      auto step = [&](int s, bool domm, bool dord, int cur) {
        const int nx = cur ^ 1;
        const unsigned b = L0 + sl + aW, ax = xaddr((2 * idx + s) & 15);
#pragma unroll
        for (int i = 0; i < 8; ++i) {
          if (dord) {
            if (i < 4) wf[nx][i] = ldtr(b ^ (i << 5), (b ^ (i << 5) ^ 128) + 4096);
            else if (i < 6) { xf[nx][2 * i - 8] = ld128(ax + (2 * i - 8) * 16384); xf[nx][2 * i - 7] = ld128(ax + (2 * i - 7) * 16384); }
          }
          if (domm) {
            acc[i >> 1][2 * (i & 1)] = mfma(wf[cur][i >> 1], xf[cur][2 * (i & 1)], acc[i >> 1][2 * (i & 1)]);
            acc[i >> 1][2 * (i & 1) + 1] = mfma(wf[cur][i >> 1], xf[cur][2 * (i & 1) + 1], acc[i >> 1][2 * (i & 1) + 1]);
          }
          SB();
        }
        if (dord) sl = nxt(sl);
      };
      cbar(); step(0, false, true, 1);
#pragma unroll 1
      for (int s = 1; s < 15; s += 2) {
        cbar(); step(s, true, true, 0);
        cbar(); step(s + 1, true, true, 1);
      }
      cbar(); step(15, true, true, 0);
      if (endbar) cbar();
      step(0, true, false, 1);
    };
    {
      const unsigned l = fresh(lane), c = l & 15, g = l >> 4;
      gemm([&](unsigned ks) { return L0 + RES_OFF + c * 1024 + (((4 * ks + g) ^ c) << 4); }, true);
    }
    STAMP(1);
    {
      const unsigned aF = AF() + ((cw >> 1) * 8 + 2 * (cw & 1)) * 1024;
#pragma unroll
      for (int nb = 0; nb < 4; ++nb)
#pragma unroll
        for (int bq = 0; bq < 4; ++bq)
          st64(aF + (bq >> 1) * 32768 + ((bq & 1) * 4 + (nb >> 1)) * 1024 + (nb & 1) * 512, cvtpk(acc[nb][bq][0], acc[nb][bq][1]), cvtpk(acc[nb][bq][2], acc[nb][bq][3]));
    }
    const unsigned aK = AK(), aV = AV();
    constexpr int PD = 6;
    u32x4 fr[8];
    auto rdK = [&](unsigned b, int i) { fr[i & 7] = ld128((b ^ ((i & 3) << 6)) + (i >> 2) * 4096); };
    auto rdV = [&](unsigned b, int i) { const unsigned ad = (b ^ ((i & 7) << 5)) + (i >> 3) * 8192; fr[i & 7] = ldtr(ad, ad + 4096); };
    cbar();
    {
      const unsigned b = L0 + sl + aK;
#pragma unroll
      for (int i = 0; i < PD; ++i) rdK(b, i);
    }
#pragma unroll 1
    for (int bt = 0; bt < 2; ++bt) {
      u32x4 qf[4];
      {
        const unsigned aq = AL() + bt * 32768 + cw * 4096;
#pragma unroll
        for (int kk = 0; kk < 4; ++kk) qf[kk] = ld128(aq + kk * 1024);
      }
      f32x4 o[8];
#pragma unroll
      for (int i = 0; i < 8; ++i) o[i] = Z4;
      float m = -INFINITY, ls = 0.f;
#pragma unroll 1
      for (int bk = 0; bk < 4; ++bk) {
        f32x4 s[8];
        u32x4 pf[4];
#pragma unroll
        for (int i = 0; i < 8; ++i) s[i] = Z4;
#pragma unroll
        for (int t = 0; t < 4; ++t) {
          const unsigned bc = L0 + sl + (t < 2 ? aK : aV);
          sl = nxth(sl);
          const unsigned bn = L0 + sl + ((t == 0 || t == 3) ? aK : aV);
          const bool last = t == 3 && bt == 1 && bk == 3;
#pragma unroll
          for (int i = 0; i < 16; ++i) {
            if (i == 16 - PD && !last) { SB(); lbar(); SB(); }
            const u32x4 f = fr[i & 7];
            if (t < 2) s[t * 4 + (i >> 2)] = mfma(f, qf[i & 3], s[t * 4 + (i >> 2)]);
            else o[i & 7] = mfma(f, pf[(t - 2) * 2 + (i >> 3)], o[i & 7]);
            if (i + PD < 16) { if (t < 2) rdK(bc, i + PD); else rdV(bc, i + PD); }
            else { if (t == 0 || t == 3) rdK(bn, i + PD - 16); else rdV(bn, i + PD - 16); }
            SB();
          }
          if (t == 1) {
            float mx = s[0][0];
#pragma unroll
            for (int kb = 0; kb < 8; ++kb)
#pragma unroll
              for (int r = 0; r < 4; ++r) mx = fmaxf(mx, s[kb][r]);
            mx = xmax4(mx);
            const float mn = fmaxf(m, mx * SL2E);
            const float alpha = __builtin_amdgcn_exp2f(m - mn);
            m = mn;
            float ps = 0.f;
#pragma unroll
            for (int kb = 0; kb < 8; ++kb) {
#pragma unroll
              for (int r = 0; r < 4; ++r) { s[kb][r] = __builtin_amdgcn_exp2f(fmaf(s[kb][r], SL2E, -mn)); ps += s[kb][r]; }
              pf[kb >> 1][2 * (kb & 1)] = cvtpk(s[kb][0], s[kb][1]);
              pf[kb >> 1][2 * (kb & 1) + 1] = cvtpk(s[kb][2], s[kb][3]);
            }
            ls = ls * alpha + ps;
#pragma unroll
            for (int i = 0; i < 8; ++i) o[i] *= alpha;
            SB();
          }
        }
      }
      const float inv = 1.f / xsum4(ls);
      const unsigned aF = AF() + ((bt * 2 + (cw & 1)) * 16 + (cw >> 1) * 4) * 1024;
#pragma unroll
      for (int db = 0; db < 8; ++db) st64(aF + db * 512, cvtpk(o[db][0] * inv, o[db][1] * inv), cvtpk(o[db][2] * inv, o[db][3] * inv));
      STAMP(2 + bt);
    }
    {
      const unsigned al = AL();
      gemm([&](unsigned ks) { return al + ks * 1024; }, false);
    }
    STAMP(4);
#pragma unroll
    for (int bq = 0; bq < 4; ++bq)
#pragma unroll
      for (int np = 0; np < 2; ++np) {
        const u32x4 v = {cvtpk(acc[2 * np][bq][0], acc[2 * np][bq][1]), cvtpk(acc[2 * np][bq][2], acc[2 * np][bq][3]),
                         cvtpk(acc[2 * np + 1][bq][0], acc[2 * np + 1][bq][1]), cvtpk(acc[2 * np + 1][bq][2], acc[2 * np + 1][bq][3])};
        __builtin_amdgcn_raw_buffer_store_b128(v, rt, lane * 16, WS_TILES_A + ((2 * idx + (bq >> 1)) * 8 + xcd) * TILE_B + ((cw * 2 + np) * 2 + (bq & 1)) * 1024, 16);
      }
    cbar_vm();
    if (cw == 0 && lane < 2) __hip_atomic_store((GAS u64*)(a.ws + WS_FLAGA + ((2 * idx + lane) * 8 + xcd) * 8), tag, RLX);
  } else {
    f32x4 oa[4][2];
#pragma unroll
    for (int nb = 0; nb < 4; ++nb) { oa[nb][0] = Z4; oa[nb][1] = Z4; }
    auto qproj = [&](bool first) {
      f32x4 acc[2] = {Z4, Z4};
      const unsigned l = fresh(lane), c = l & 15, g = l >> 4, qq = c >> 2;
      const unsigned aq = (8 * g + qq) * 256 + (((unsigned)cw ^ (qq ^ (4 * (g & 1)))) << 5) + 8 * (l & 3);
      const unsigned ax0 = L0 + X_OFF + c * 1024 + ((g ^ c) << 4);
      u32x4 xf[2][8];
      auto rdx = [&](int st) {
        const unsigned ax = ax0 + ((st + bB) & 3) * 256;
#pragma unroll
        for (int i = 0; i < 8; ++i) xf[st & 1][i] = ld128((ax ^ ((i >> 1) << 6)) + (i & 1) * 16384);
      };
      if (!first) rdx(0);
#pragma unroll
      for (int st = 0; st < 4; ++st) {
        cbar();
        const unsigned b = L0 + sl + aq;
        u32x4 wf[4];
#pragma unroll
        for (int ks = 0; ks < 4; ++ks) wf[ks] = ldtr(b + ks * 8192, (b ^ 128) + 1024 + ks * 8192);
        if (first && st == 0) rdx(0);
        cbar();
        if (st < 3) rdx(st + 1);
        SB();
#pragma unroll
        for (int i = 0; i < 8; ++i) acc[i & 1] = mfma(wf[i >> 1], xf[st & 1][i], acc[i & 1]);
        sl = nxt(sl);
      }
#pragma unroll
      for (int qb = 0; qb < 2; ++qb) st64(AF() + cw * 512 + (Q_OFF - RING) + qb * 4096, cvtpk(acc[qb][0], acc[qb][1]), cvtpk(acc[qb][2], acc[qb][3]));
    };
    qproj(true);
    STAMP(8);
#pragma unroll 1
    for (int u = 0; u < nu; ++u) {
      f32x4 s[4][2];
      {
        u32x4 qf[2][4];
        const unsigned aK = AK() + cw * 4096;
#pragma unroll
        for (int dl = 0; dl < 4; ++dl) {
          cbar();
          if (u == 1 && (dl == 0 || dl == 3)) STAMP(9 + dl);
          const unsigned b = L0 + sl + aK;
          u32x4 kf[4];
#pragma unroll
          for (int kk = 0; kk < 4; ++kk) kf[kk] = ld128(b ^ (kk << 6));
          if (dl == 0) {
            const unsigned al = AL();
#pragma unroll
            for (int qb = 0; qb < 2; ++qb)
#pragma unroll
              for (int kk = 0; kk < 4; ++kk) qf[qb][kk] = ld128(al + (Q_OFF - RING) + (qb * 4 + kk) * 1024);
          }
          cbar();
          s[dl][0] = Z4; s[dl][1] = Z4;
#pragma unroll
          for (int kk = 0; kk < 4; ++kk) { s[dl][0] = mfma(kf[kk], qf[0][kk], s[dl][0]); s[dl][1] = mfma(kf[kk], qf[1][kk], s[dl][1]); }
          sl = nxt(sl);
        }
      }
      if (u == 0) STAMP(1);
      const unsigned aS = L0 + ST_OFF + (fresh(lane) & 15) * 32;
#pragma unroll
      for (int qb = 0; qb < 2; ++qb) {
        float mx = s[0][qb][0];
#pragma unroll
        for (int dl = 0; dl < 4; ++dl)
#pragma unroll
          for (int r = 0; r < 4; ++r) mx = fmaxf(mx, s[dl][qb][r]);
        mx = xmax4(mx);
        if (lane < 16) *(LAS float*)(uintptr_t)(aS + qb * 512 + cw * 4) = mx;
      }
      cbar();
      u32x2 pk[2][2];
      const unsigned oP = (P_OFF - RING) + (cw >> 1) * 2048 + (cw & 1) * 8;
      {
        const unsigned aP = AL() + oP;
#pragma unroll
        for (int qb = 0; qb < 2; ++qb) {
          const f32x4 m0 = ldf4(aS + qb * 512), m1 = ldf4(aS + qb * 512 + 16);
          const float mn = SL2E * fmaxf(fmaxf(fmaxf(m0[0], m0[1]), fmaxf(m0[2], m0[3])), fmaxf(fmaxf(m1[0], m1[1]), fmaxf(m1[2], m1[3])));
          float ps = 0.f;
#pragma unroll
          for (int dl = 0; dl < 4; ++dl) {
#pragma unroll
            for (int r = 0; r < 4; ++r) { s[dl][qb][r] = __builtin_amdgcn_exp2f(fmaf(s[dl][qb][r], SL2E, -mn)); ps += s[dl][qb][r]; }
            const unsigned lo = cvtpk(s[dl][qb][0], s[dl][qb][1]), hi = cvtpk(s[dl][qb][2], s[dl][qb][3]);
            if (dl < 2) st64(aP + dl * 8192 + qb * 1024, lo, hi); else pk[dl - 2][qb] = u32x2{lo, hi};
          }
          ps = xsum4(ps);
          if (lane < 16) *(LAS float*)(uintptr_t)(aS + 1024 + qb * 512 + cw * 4) = ps;
        }
      }
      f32x4 o[2] = {Z4, Z4};
      auto vstep = [&](int dl, bool pre) {
        const unsigned ap = AL() + (P_OFF - RING) + (dl & 1) * 8192;
        u32x4 pf[4][2], vf[4];
        if (pre) {
#pragma unroll
          for (int i = 0; i < 8; ++i) pf[i >> 1][i & 1] = ld128(ap + i * 1024);
        }
        cbar();
        if (u == 1 && dl >= 2) STAMP(dl == 2 ? 16 : 22);
        const unsigned b = (L0 + sl + AV()) ^ ((unsigned)cw << 5);
#pragma unroll
        for (int ks = 0; ks < 4; ++ks) vf[ks] = ldtr(b + ks * 8192, b + ks * 8192 + 4096);
        if (!pre) {
#pragma unroll
          for (int i = 0; i < 8; ++i) pf[i >> 1][i & 1] = ld128(ap + i * 1024);
        }
        cbar();
#pragma unroll
        for (int ks = 0; ks < 4; ++ks)
#pragma unroll
          for (int qb = 0; qb < 2; ++qb) o[qb] = mfma(vf[ks], pf[ks][qb], o[qb]);
        sl = nxt(sl);
      };
      vstep(0, false);
      vstep(1, true);
      {
        const unsigned aP = AL() + oP;
#pragma unroll
        for (int i = 0; i < 2; ++i)
#pragma unroll
          for (int qb = 0; qb < 2; ++qb) st64(aP + i * 8192 + qb * 1024, pk[i][qb][0], pk[i][qb][1]);
      }
      float inv[2];
#pragma unroll
      for (int qb = 0; qb < 2; ++qb) {
        const f32x4 l0 = ldf4(aS + 1024 + qb * 512), l1 = ldf4(aS + 1024 + qb * 512 + 16);
        inv[qb] = 1.f / (((l0[0] + l0[1]) + (l0[2] + l0[3])) + ((l1[0] + l1[1]) + (l1[2] + l1[3])));
      }
      vstep(2, false);
      if (u + 1 < nu) qproj(false);
      if (u == 1) STAMP(20);
      u32x4 wo[4][4];
#pragma unroll
      for (int kk = 0; kk < 4; ++kk) {
        cbar();
        const unsigned b = L0 + sl + AW();
#pragma unroll
        for (int nb = 0; nb < 4; ++nb) wo[kk][nb] = ldtr(b ^ (nb << 5), (b ^ (nb << 5) ^ 128) + 4096);
        cbar();
        sl = nxt(sl);
      }
      vstep(3, true);
#pragma unroll
      for (int qb = 0; qb < 2; ++qb) {
        const f32x4 ov = o[qb] * inv[qb];
        st64(AF() + cw * 512 + (P_OFF - RING) + qb * 4096, cvtpk(ov[0], ov[1]), cvtpk(ov[2], ov[3]));
      }
      cbar();
      {
        u32x4 of[2][4];
        const unsigned al = AL() + (P_OFF - RING);
#pragma unroll
        for (int kk = 0; kk < 4; ++kk)
#pragma unroll
          for (int qb = 0; qb < 2; ++qb) of[qb][kk] = ld128(al + ((kk + bB) & 3) * 1024 + qb * 4096);
        SB();
#pragma unroll
        for (int kk = 0; kk < 4; ++kk)
#pragma unroll
          for (int qb = 0; qb < 2; ++qb)
#pragma unroll
            for (int nb = 0; nb < 4; ++nb) oa[nb][qb] = mfma(wo[kk][nb], of[qb][kk], oa[nb][qb]);
      }
      STAMP(2 + u);
    }
#pragma unroll
    for (int qb = 0; qb < 2; ++qb)
#pragma unroll
      for (int np = 0; np < 2; ++np) {
        const u32x4 v = {cvtpk(oa[2 * np][qb][0], oa[2 * np][qb][1]), cvtpk(oa[2 * np][qb][2], oa[2 * np][qb][3]),
                         cvtpk(oa[2 * np + 1][qb][0], oa[2 * np + 1][qb][1]), cvtpk(oa[2 * np + 1][qb][2], oa[2 * np + 1][qb][3])};
        __builtin_amdgcn_raw_buffer_store_b128(v, rt, lane * 16, (bB * 12 + slotB) * TILE_B + ((cw * 2 + np) * 2 + qb) * 1024, 16);
      }
    cbar_vm();
    if (cw == 0 && lane == 0) __hip_atomic_store((GAS u64*)(a.ws + WS_FLAGB + (bB * 16 + slotB) * 8), tag, RLX);
  }
  STAMP(5);

  {
    const unsigned c = lane & 15, g = lane >> 4;
    const int rb = wg >> 4, pc = (wg & 15) * 2 + ((cw >> 1) & 1), tg = cw & 1;
    const bool wB = cw < 4;
    const unsigned nt = wB ? 6 : 4;
    const unsigned fl = lane < nt ? lane : 0;
    const GAS u64* fp = (const GAS u64*)(a.ws + (wB ? WS_FLAGB + (rb * 16 + tg * 6 + fl) * 8 : WS_FLAGA + (rb * 8 + tg * 4 + fl) * 8));
#define NSPIN (1 << 16)
    for (int spin = 0; spin < NSPIN; ++spin) {
      if (__all(__hip_atomic_load(fp, RLX) == tag)) break;
      __builtin_amdgcn_s_sleep(4);
    }
    __builtin_amdgcn_fence(__ATOMIC_ACQUIRE, "wavefront");
    STAMP(6);
    const unsigned so = (wB ? (rb * 12 + tg * 6) * TILE_B : WS_TILES_A + (rb * 8 + tg * 4) * TILE_B) + pc * 1024;
    u32x4 t[6];
#pragma unroll
    for (int i = 0; i < 6; ++i) t[i] = __builtin_amdgcn_raw_buffer_load_b128(rt, (i < 4 || wB) ? lane * 16 : 0x7fffff00u, so + i * TILE_B, 16);
    const f32x4 s0 = ((bflo(t[0]) + bflo(t[1])) + (bflo(t[2]) + bflo(t[3]))) + (bflo(t[4]) + bflo(t[5]));
    const f32x4 s1 = ((bfhi(t[0]) + bfhi(t[1])) + (bfhi(t[2]) + bfhi(t[3]))) + (bfhi(t[4]) + bfhi(t[5]));
    *(LAS f32x4*)(uintptr_t)(L0 + cw * 1024 + lane * 16) = tg ? s0 : s1;
    cbar();
    const f32x4 oth = ldf4(L0 + (cw ^ 1) * 1024 + lane * 16);
    const f32x4 r = tg ? oth + s1 : s0 + oth;
    *(GAS u32x2*)(a.out + ((size_t)(rb * SQ + (pc & 1) * 16 + c) * 1024 + (wB ? 512 : 0) + 32 * (pc >> 1) + 16 * tg + 4 * g) * 2) = u32x2{cvtpk(r[0], r[1]), cvtpk(r[2], r[3])};
    if (wg == 255 && cw == 1) {
      for (int spin = 0; spin < NSPIN; ++spin) {
        bool ok = true;
#pragma unroll
        for (int i = 0; i < 4; ++i) ok &= (lane & 15) >= 12 || __hip_atomic_load((const GAS u64*)(a.ws + WS_FLAGB + (i * 64 + lane) * 8), RLX) == tag;
#pragma unroll
        for (int i = 0; i < 2; ++i) ok &= __hip_atomic_load((const GAS u64*)(a.ws + WS_FLAGA + (i * 64 + lane) * 8), RLX) == tag;
        if (__all(ok)) break;
        __builtin_amdgcn_s_sleep(2);
      }
      if (lane == 0) __hip_atomic_store((GAS u64*)a.ws, tag, RLX);
    }
  }
  STAMP(7);
  if (cw == 0 && lane == 0) { a.stamps[wg * 32 + 31] = __builtin_amdgcn_s_memtime() - clk0;
    _Pragma("unroll") for (int k_ = 0; k_ < 30; ++k_) a.stamps[wg * 32 + k_] = stv[k_]; }
}

__global__ void spin_k(const u64* st) {
  __shared__ unsigned red[3][256];
  const int i = threadIdx.x;
  const unsigned t0 = (unsigned)st[i * 32], tk = (unsigned)st[i * 32 + 7];
  red[0][i] = t0; red[1][i] = tk;
  __syncthreads();
  if (i == 0) {
    unsigned mn = red[0][0];
    for (int j = 0; j < 256; ++j) mn = (int)(red[0][j] - mn) < 0 ? red[0][j] : mn;
    unsigned v = 0;
    if (1) { for (int j = 0; j < 256; ++j) v = max(v, red[1][j] - mn); }
    else { for (int j = 64; j < 192; ++j) v += red[1][j] - mn; v /= 128; }
    v = 0;
    const u64 s0 = __builtin_amdgcn_s_memrealtime();
    while (__builtin_amdgcn_s_memrealtime() - s0 < 20ull * v) __builtin_amdgcn_s_sleep(4);
  }
}
extern "C" void kernel_launch(void* const* d_in, const int* in_sizes, int n_in, void* d_out, int out_size, void* d_ws, size_t ws_size,
                              hipStream_t stream) {
  Args a;
  for (int br = 0; br < 2; ++br) {
    a.x[br] = d_in[5 * br + 0];
    a.k[br] = d_in[5 * br + 1];
    a.v[br] = d_in[5 * br + 2];
    a.wq[br] = d_in[5 * br + 3];
    a.wo[br] = d_in[5 * br + 4];
  }
  a.ws = (char*)d_ws;
  a.out = (char*)d_out;
  a.stamps = (u64*)((char*)d_ws + (64u << 20));
  (void)hipFuncSetAttribute((const void*)fused_k, hipFuncAttributeMaxDynamicSharedMemorySize, LDS_BYTES);
  fused_k<<<256, 768, LDS_BYTES, stream>>>(a);
  spin_k<<<1, 256, 0, stream>>>(a.stamps);
}
```

```cpp
#include <hip/hip_runtime.h>
#include <stdint.h>

#define LAS __attribute__((address_space(3)))
#define GAS __attribute__((address_space(1)))
typedef unsigned u32x4 __attribute__((ext_vector_type(4)));
typedef unsigned u32x2 __attribute__((ext_vector_type(2)));
typedef float f32x4 __attribute__((ext_vector_type(4)));
typedef short bf16x8 __attribute__((ext_vector_type(8)));
typedef short s16x4 __attribute__((ext_vector_type(4)));
typedef __amdgpu_buffer_rsrc_t rsrc_t;
typedef unsigned long long u64;

constexpr int NB = 16, SQ = 32, DM = 512, NH = 32, SK = 512, DH = 128;
constexpr float SL2E = 0.088388347648318447f * 1.4426950408889634f;
constexpr unsigned DT = 32768;
constexpr unsigned RING = 3 * DT;
constexpr unsigned X_OFF = RING, P_OFF = RING + 32768, Q_OFF = P_OFF + 16384, ST_OFF = Q_OFF + 8192;
constexpr unsigned RES_OFF = RING;
constexpr unsigned LDS_BYTES = 160 * 1024;
constexpr int NLD = 4;
constexpr unsigned WS_FLAGB = 4096, WS_FLAGA = 8192 + 4096, WS_TILES = 1u << 20;
constexpr unsigned TILE_B = 32768;
constexpr unsigned WS_TILES_A = 192 * TILE_B;
constexpr unsigned WS_TILES_BYTES = 320 * TILE_B;

struct Args {
  const void* x[2]; const void* k[2]; const void* v[2]; const void* wq[2]; const void* wo[2];
  char* ws; char* out;
};

__device__ __forceinline__ rsrc_t mkrsrc(const void* p, unsigned bytes) { return __builtin_amdgcn_make_buffer_rsrc((void*)p, 0, bytes, 0x00020000); }
template <int AUX> __device__ __forceinline__ void dma(rsrc_t r, unsigned lds, unsigned voff, unsigned soff) {
  __builtin_amdgcn_raw_ptr_buffer_load_lds(r, (LAS void*)(uintptr_t)lds, 16, voff, soff, 0, AUX);
}
template <int N> __device__ __forceinline__ void waitb() { asm volatile("s_waitcnt vmcnt(%0)\n\ts_barrier" :: "i"(N) : "memory"); }
__device__ __forceinline__ void lbar() { asm volatile("s_barrier" ::: "memory"); }
__device__ __forceinline__ void cbar() { __builtin_amdgcn_sched_barrier(0); asm volatile("s_waitcnt lgkmcnt(0)\n\ts_barrier" ::: "memory"); __builtin_amdgcn_sched_barrier(0); }
__device__ __forceinline__ void rbar() { __builtin_amdgcn_sched_barrier(0); asm volatile("s_barrier" ::: "memory"); __builtin_amdgcn_sched_barrier(0); }
__device__ __forceinline__ void cbar_vm() { __builtin_amdgcn_sched_barrier(0); asm volatile("s_waitcnt vmcnt(0) lgkmcnt(0)\n\ts_barrier" ::: "memory"); __builtin_amdgcn_sched_barrier(0); }

__device__ __forceinline__ f32x4 mfma(u32x4 a, u32x4 b, f32x4 c) {
  return __builtin_amdgcn_mfma_f32_16x16x32_bf16(__builtin_bit_cast(bf16x8, a), __builtin_bit_cast(bf16x8, b), c, 0, 0, 0);
}
typedef float f32x2 __attribute__((ext_vector_type(2)));
typedef __bf16 bf16x2 __attribute__((ext_vector_type(2)));
__device__ __forceinline__ unsigned cvtpk(float lo, float hi) {
  return __builtin_bit_cast(unsigned, __builtin_convertvector(f32x2{lo, hi}, bf16x2));
}
__device__ __forceinline__ u32x4 ld128(unsigned a) {
  return *(const LAS u32x4*)(uintptr_t)a; }
__device__ __forceinline__ f32x4 ldf4(unsigned a) { return *(const LAS f32x4*)(uintptr_t)a; }
__device__ __forceinline__ void st64(unsigned a, unsigned lo, unsigned hi) { *(LAS u32x2*)(uintptr_t)a = u32x2{lo, hi}; }
__device__ __forceinline__ u32x4 ldtr(unsigned a0, unsigned a1) {
  const u32x2 lo = __builtin_bit_cast(u32x2, __builtin_amdgcn_ds_read_tr16_b64_v4i16((LAS s16x4*)(uintptr_t)a0));
  const u32x2 hi = __builtin_bit_cast(u32x2, __builtin_amdgcn_ds_read_tr16_b64_v4i16((LAS s16x4*)(uintptr_t)a1));
  return u32x4{lo[0], lo[1], hi[0], hi[1]};
}
__device__ __forceinline__ float xmax4(float x) {
  auto s = __builtin_amdgcn_permlane16_swap(__float_as_uint(x), __float_as_uint(x), false, false);
  x = fmaxf(__uint_as_float(s[0]), __uint_as_float(s[1]));
  auto t = __builtin_amdgcn_permlane32_swap(__float_as_uint(x), __float_as_uint(x), false, false);
  return fmaxf(__uint_as_float(t[0]), __uint_as_float(t[1]));
}
__device__ __forceinline__ float xsum4(float x) {
  auto s = __builtin_amdgcn_permlane16_swap(__float_as_uint(x), __float_as_uint(x), false, false);
  x = __uint_as_float(s[0]) + __uint_as_float(s[1]);
  auto t = __builtin_amdgcn_permlane32_swap(__float_as_uint(x), __float_as_uint(x), false, false);
  return __uint_as_float(t[0]) + __uint_as_float(t[1]);
}
__device__ __forceinline__ unsigned nxt(unsigned s) { s += DT; return s == RING ? 0u : s; }
__device__ __forceinline__ unsigned nxth(unsigned s) { s += DT / 2; return s == RING ? 0u : s; }
#define ABL_OFF 0
#define SB() __builtin_amdgcn_sched_barrier(0)
__device__ __forceinline__ unsigned fresh(unsigned x) { asm volatile("" : "+v"(x)); return x; }
#define RLX __ATOMIC_RELAXED, __HIP_MEMORY_SCOPE_AGENT
#define KFIRST_MASK 0x2a
__device__ __forceinline__ f32x4 bflo(u32x4 v) { return f32x4{__uint_as_float(v[0] << 16), __uint_as_float(v[0] & 0xffff0000u), __uint_as_float(v[1] << 16), __uint_as_float(v[1] & 0xffff0000u)}; }
__device__ __forceinline__ f32x4 bfhi(u32x4 v) { return f32x4{__uint_as_float(v[2] << 16), __uint_as_float(v[2] & 0xffff0000u), __uint_as_float(v[3] << 16), __uint_as_float(v[3] & 0xffff0000u)}; }

#define Z4 f32x4{0.f, 0.f, 0.f, 0.f}
#define STAMP(i) do {} while (0)

__global__ void __launch_bounds__(768) fused_k(Args a) {
  extern __shared__ __attribute__((aligned(16))) char smem[];
  const unsigned lane = threadIdx.x & 63;
  const int w = __builtin_amdgcn_readfirstlane(threadIdx.x >> 6);
  const int wg = blockIdx.x, xcd = wg & 7, idx = wg >> 3;
  const bool isA = idx < 8;
  const int nu = idx < 24 ? 3 : 2;
  const int bB = nu == 3 ? idx - 8 : (xcd & 1) * 8 + (idx - 24);
  const int slotB = nu == 3 ? xcd : 8 + (xcd >> 1);
  auto headB = [&](int u) { int x = u + bB % 3; x = x >= 3 ? x - 3 : x; return nu == 3 ? 4 * xcd + x : 4 * (xcd ^ u ^ (bB & 1)) + 3; };
#define PVB (4 + (idx & 3))
  const int pvB = PVB;
  const unsigned L0 = (unsigned)(uintptr_t)(LAS char*)smem;

  if (w < NLD) {
    __builtin_amdgcn_s_setprio(3);
    const unsigned l = w;
    const unsigned e4 = lane >> 4, s16 = lane & 15, r15 = 4 * l + e4;
    const unsigned fT = (r15 & 7) ^ ((r15 & 8) >> 1);
    const unsigned vK = r15 * 256 + ((s16 ^ r15) << 4);
    const unsigned vV = r15 * 256 + ((s16 ^ (fT << 1)) << 4);
    const unsigned vW0 = (lane * 16) ^ (l * 32), vW1 = vW0 ^ 128;
    const unsigned pl = L0 + l * 1024;
    unsigned sl = 0;
    auto kvtile = [&](rsrc_t r, unsigned voff, unsigned so) {
#pragma unroll
      for (int q = 0; q < 8; ++q) dma<2>(r, pl + sl + q * 4096, voff, so + q * 4096);
      sl = nxt(sl);
    };
    auto kvhalf = [&](rsrc_t r, unsigned voff, unsigned so) {
#pragma unroll
      for (int q = 0; q < 4; ++q) dma<2>(r, pl + sl + q * 4096, voff, so + q * 4096);
      sl = nxth(sl);
    };
    auto whalf = [&](rsrc_t r, unsigned so, unsigned rs) {
#pragma unroll
      for (int q = 0; q < 4; ++q) dma<0>(r, pl + sl + q * 4096, ((q ^ (q >> 1)) & 1) ? vW1 : vW0, so + q * 4 * rs);
      sl = nxth(sl);
    };
    auto wtile = [&](rsrc_t r, unsigned so, unsigned rs) { whalf(r, so, rs); whalf(r, so + 16 * rs, rs); };
    if (isA) {
      const rsrc_t rx = mkrsrc(a.x[0], NB * SQ * DM * 2), rk = mkrsrc(a.k[0], NB * 8 * SK * DH * 2), rv = mkrsrc(a.v[0], NB * 8 * SK * DH * 2);
      const rsrc_t rq = mkrsrc(a.wq[0], DM * NH * DH * 2), ro = mkrsrc(a.wo[0], DM * NH * DH * 2);
      const unsigned vX = (lane ^ l) * 16;
      const unsigned xo = (unsigned)idx * (2 * SQ * DM * 2) + l * 1024;
      const unsigned qo = xcd * 1024 + l * 8192;
#pragma unroll
      for (int q = 0; q < 16; ++q) dma<0>(rx, pl + RES_OFF + q * 4096, vX ^ ((q & 3) * 64), xo + q * 4096);
      wtile(rq, qo + ((2 * idx) & 15) * (32 * 8192), 8192);
      wtile(rq, qo + ((2 * idx + 1) & 15) * (32 * 8192), 8192);
#pragma unroll 1
      for (int s = 2; s < 16; ++s) { waitb<8>(); wtile(rq, qo + ((2 * idx + s) & 15) * (32 * 8192), 8192); }
      const unsigned kvo = (unsigned)(2 * idx * 8 + xcd) * (SK * DH * 2);
      waitb<8>(); kvtile(rk, vK, kvo);
      waitb<8>(); kvtile(rv, vV, kvo);
      lbar();
      kvtile(rk, vK, kvo + DT);
      waitb<20>();
      waitb<16>();
#pragma unroll 1
      for (int bk = 1; bk < 8; ++bk) {
        const unsigned so = kvo + (bk >> 2) * (8 * SK * DH * 2) + (bk & 3) * DT;
        waitb<12>(); kvhalf(rv, vV, so);
        waitb<12>(); kvhalf(rv, vV, so + DT / 2);
        if (bk == 7) break;
        const unsigned sn = kvo + ((bk + 1) >> 2) * (8 * SK * DH * 2) + ((bk + 1) & 3) * DT;
        waitb<12>(); kvhalf(rk, vK, sn);
        waitb<12>(); kvhalf(rk, vK, sn + DT / 2);
      }
      const unsigned oo = xcd * (512 * 1024) + l * 1024;
      const unsigned o0 = oo + ((2 * idx) & 15) * DT, o1 = oo + ((2 * idx + 1) & 15) * DT, o2 = oo + ((2 * idx + 2) & 15) * DT;
      waitb<12>(); whalf(ro, o0, 1024);
      waitb<12>(); whalf(ro, o0 + DT / 2, 1024);
      waitb<12>(); whalf(ro, o1, 1024);
      waitb<12>(); whalf(ro, o1 + DT / 2, 1024);
      waitb<8>(); wtile(ro, o2, 1024);
#pragma unroll 1
      for (int s = 3; s < 16; ++s) { waitb<8>(); wtile(ro, oo + ((2 * idx + s) & 15) * DT, 1024); }
      waitb<8>();
      waitb<0>();
    } else {
      const rsrc_t rx = mkrsrc(a.x[1], NB * SQ * DM * 2), rk = mkrsrc(a.k[1], NB * NH * SK * DH * 2), rv = mkrsrc(a.v[1], NB * NH * SK * DH * 2);
      const rsrc_t rq = mkrsrc(a.wq[1], DM * NH * DH * 2), ro = mkrsrc(a.wo[1], DM * NH * DH * 2);
      const unsigned vWq = r15 * 8192 + ((s16 ^ (fT << 1)) << 4);
      const unsigned vX = (lane ^ l) * 16;
      const unsigned r7 = 2 * l + (lane >> 5);
      const unsigned vO0 = r7 * 1024 + (((lane & 31) * 16) ^ (r7 * 32)), vO1 = vO0 ^ 128;
      auto qhalf = [&](int h, int i) {
        const unsigned so = ((i + bB) & 7) * (64 * 8192) + h * 256;
#pragma unroll
        for (int q = 0; q < 4; ++q) dma<0>(rq, pl + sl + q * 4096, vWq, so + q * (16 * 8192));
        sl = nxth(sl);
      };
      auto ohalf = [&](int h, int i) {
        const unsigned so = h * (128 * 1024) + (((i >> 1) + bB) & 3) * DT + (i & 1) * 512;
#pragma unroll
        for (int q = 0; q < 4; ++q) dma<0>(ro, pl + sl + q * 4096, (q & 1) ? vO1 : vO0, so + q * 8192);
        sl = nxth(sl);
      };
      int h = headB(0);
#pragma unroll
      for (int q = 0; q < 8; ++q) dma<0>(rx, pl + X_OFF + q * 4096, vX ^ ((q & 3) * 64), (unsigned)bB * (SQ * DM * 2) + l * 1024 + q * 4096);
#pragma unroll 1
      for (int i = 0; i < 4; ++i) qhalf(h, i);
#pragma unroll 1
      for (int i = 4; i < 8; ++i) { waitb<12>(); qhalf(h, i); }
#pragma unroll 1
      for (int u = 0; u < nu; ++u) {
        const unsigned kvo = (unsigned)(bB * NH + h) * (SK * DH * 2);
#pragma unroll 1
        for (int i = 0; i < 8; ++i) { waitb<12>(); if (i == 3 && u > 0) lbar(); kvhalf(rk, vK, kvo + i * (DT / 2)); }
#pragma unroll 1
        for (int i = 0; i < pvB; ++i) { waitb<12>(); if (i == 3) lbar(); kvhalf(rv, vV, kvo + i * (DT / 2)); }
        const int hn = headB(u + 1);
        if (u + 1 < nu) {
#pragma unroll 2
          for (int i = 0; i < 8; ++i) { waitb<12>(); qhalf(hn, i); }
        }
#pragma unroll 2
        for (int i = 0; i < 8; ++i) { waitb<12>(); ohalf(h, i); }
#pragma unroll 1
        for (int i = pvB; i < 8; ++i) { waitb<12>(); kvhalf(rv, vV, kvo + i * (DT / 2)); }
        h = hn;
      }
      waitb<12>(); waitb<8>(); waitb<4>(); waitb<0>();
    }
    return;
  }

  const int cw = w - NLD;
  STAMP(0);
  const u64 ep = __hip_atomic_load((const GAS u64*)a.ws, RLX);
  const u64 tag = (((u64)(unsigned)__builtin_amdgcn_readfirstlane((unsigned)(ep >> 32)) << 32) | (unsigned)__builtin_amdgcn_readfirstlane((unsigned)ep)) + 0x9E3779B97F4A7C15ull;
  const rsrc_t rt = mkrsrc(a.ws + WS_TILES, WS_TILES_BYTES);
  unsigned sl = 0;
  auto AK = [&]() { const unsigned l = fresh(lane), c = l & 15, g = l >> 4; return c * 256 + ((g ^ c) << 4); };
  auto AV = [&]() { const unsigned l = fresh(lane), g = l >> 4, qq = (l >> 2) & 3;
                    return (4 * g + qq) * 256 + (((4 * (g & 1) + qq) ^ (4 * (g >> 1))) << 5) + 8 * (l & 3); };
  auto AW = [&]() { const unsigned l = fresh(lane), g = l >> 4, qq = (l >> 2) & 3;
                    return (8 * g + qq) * 1024 + ((((unsigned)cw * 4) ^ (qq ^ (4 * (g & 1)))) << 5) + 8 * (l & 3); };
  auto AF = [&]() { const unsigned l = fresh(lane); return L0 + RING + ((l & 15) + 16 * (l >> 5)) * 16 + 8 * ((l >> 4) & 1); };
  auto AL = [&]() { return L0 + RING + fresh(lane) * 16; };

  if (isA) {
    f32x4 acc[4][4];
    auto gemm = [&](auto xaddr, bool endbar) {
#pragma unroll
      for (int i = 0; i < 4; ++i)
#pragma unroll
        for (int j = 0; j < 4; ++j) acc[i][j] = Z4;
      u32x4 xf[2][4], wf[2][4];
      const unsigned aW = AW();
      auto step = [&](int s, bool domm, bool dord, int cur) {
        const int nx = cur ^ 1;
        const unsigned b = L0 + sl + aW, ax = xaddr((2 * idx + s) & 15);
#pragma unroll
        for (int i = 0; i < 8; ++i) {
          if (dord) {
            if (i < 4) wf[nx][i] = ldtr(b ^ (i << 5), (b ^ (i << 5) ^ 128) + 4096);
            else if (i < 6) { xf[nx][2 * i - 8] = ld128(ax + (2 * i - 8) * 16384); xf[nx][2 * i - 7] = ld128(ax + (2 * i - 7) * 16384); }
          }
          if (domm) {
            acc[i >> 1][2 * (i & 1)] = mfma(wf[cur][i >> 1], xf[cur][2 * (i & 1)], acc[i >> 1][2 * (i & 1)]);
            acc[i >> 1][2 * (i & 1) + 1] = mfma(wf[cur][i >> 1], xf[cur][2 * (i & 1) + 1], acc[i >> 1][2 * (i & 1) + 1]);
          }
          SB();
        }
        if (dord) sl = nxt(sl);
      };
      cbar(); step(0, false, true, 1);
#pragma unroll 1
      for (int s = 1; s < 15; s += 2) {
        cbar(); step(s, true, true, 0);
        cbar(); step(s + 1, true, true, 1);
      }
      cbar(); step(15, true, true, 0);
      if (endbar) cbar();
      step(0, true, false, 1);
    };
    {
      const unsigned l = fresh(lane), c = l & 15, g = l >> 4;
      gemm([&](unsigned ks) { return L0 + RES_OFF + c * 1024 + (((4 * ks + g) ^ c) << 4); }, true);
    }
    STAMP(1);
    {
      const unsigned aF = AF() + ((cw >> 1) * 8 + 2 * (cw & 1)) * 1024;
#pragma unroll
      for (int nb = 0; nb < 4; ++nb)
#pragma unroll
        for (int bq = 0; bq < 4; ++bq)
          st64(aF + (bq >> 1) * 32768 + ((bq & 1) * 4 + (nb >> 1)) * 1024 + (nb & 1) * 512, cvtpk(acc[nb][bq][0], acc[nb][bq][1]), cvtpk(acc[nb][bq][2], acc[nb][bq][3]));
    }
    const unsigned aK = AK(), aV = AV();
    constexpr int PD = 6;
    u32x4 fr[8];
    auto rdK = [&](unsigned b, int i) { fr[i & 7] = ld128((b ^ ((i & 3) << 6)) + (i >> 2) * 4096); };
    auto rdV = [&](unsigned b, int i) { const unsigned ad = (b ^ ((i & 7) << 5)) + (i >> 3) * 8192; fr[i & 7] = ldtr(ad, ad + 4096); };
    cbar();
    {
      const unsigned b = L0 + sl + aK;
#pragma unroll
      for (int i = 0; i < PD; ++i) rdK(b, i);
    }
#pragma unroll 1
    for (int bt = 0; bt < 2; ++bt) {
      u32x4 qf[4];
      {
        const unsigned aq = AL() + bt * 32768 + cw * 4096;
#pragma unroll
        for (int kk = 0; kk < 4; ++kk) qf[kk] = ld128(aq + kk * 1024);
      }
      f32x4 o[8];
#pragma unroll
      for (int i = 0; i < 8; ++i) o[i] = Z4;
      float m = -INFINITY, ls = 0.f;
#pragma unroll 1
      for (int bk = 0; bk < 4; ++bk) {
        f32x4 s[8];
        u32x4 pf[4];
#pragma unroll
        for (int i = 0; i < 8; ++i) s[i] = Z4;
#pragma unroll
        for (int t = 0; t < 4; ++t) {
          const unsigned bc = L0 + sl + (t < 2 ? aK : aV);
          sl = nxth(sl);
          const unsigned bn = L0 + sl + ((t == 0 || t == 3) ? aK : aV);
          const bool last = t == 3 && bt == 1 && bk == 3;
#pragma unroll
          for (int i = 0; i < 16; ++i) {
            if (i == 16 - PD && !last) { SB(); lbar(); SB(); }
            const u32x4 f = fr[i & 7];
            if (t < 2) s[t * 4 + (i >> 2)] = mfma(f, qf[i & 3], s[t * 4 + (i >> 2)]);
            else o[i & 7] = mfma(f, pf[(t - 2) * 2 + (i >> 3)], o[i & 7]);
            if (i + PD < 16) { if (t < 2) rdK(bc, i + PD); else rdV(bc, i + PD); }
            else { if (t == 0 || t == 3) rdK(bn, i + PD - 16); else rdV(bn, i + PD - 16); }
            SB();
          }
          if (t == 1) {
            float mx = s[0][0];
#pragma unroll
            for (int kb = 0; kb < 8; ++kb)
#pragma unroll
              for (int r = 0; r < 4; ++r) mx = fmaxf(mx, s[kb][r]);
            mx = xmax4(mx);
            const float mn = fmaxf(m, mx * SL2E);
            const float alpha = __builtin_amdgcn_exp2f(m - mn);
            m = mn;
            float ps = 0.f;
#pragma unroll
            for (int kb = 0; kb < 8; ++kb) {
#pragma unroll
              for (int r = 0; r < 4; ++r) { s[kb][r] = __builtin_amdgcn_exp2f(fmaf(s[kb][r], SL2E, -mn)); ps += s[kb][r]; }
              pf[kb >> 1][2 * (kb & 1)] = cvtpk(s[kb][0], s[kb][1]);
              pf[kb >> 1][2 * (kb & 1) + 1] = cvtpk(s[kb][2], s[kb][3]);
            }
            ls = ls * alpha + ps;
#pragma unroll
            for (int i = 0; i < 8; ++i) o[i] *= alpha;
            SB();
          }
        }
      }
      const float inv = 1.f / xsum4(ls);
      const unsigned aF = AF() + ((bt * 2 + (cw & 1)) * 16 + (cw >> 1) * 4) * 1024;
#pragma unroll
      for (int db = 0; db < 8; ++db) st64(aF + db * 512, cvtpk(o[db][0] * inv, o[db][1] * inv), cvtpk(o[db][2] * inv, o[db][3] * inv));
      STAMP(2 + bt);
    }
    {
      const unsigned al = AL();
      gemm([&](unsigned ks) { return al + ks * 1024; }, false);
    }
    STAMP(4);
#pragma unroll
    for (int bq = 0; bq < 4; ++bq)
#pragma unroll
      for (int np = 0; np < 2; ++np) {
        const u32x4 v = {cvtpk(acc[2 * np][bq][0], acc[2 * np][bq][1]), cvtpk(acc[2 * np][bq][2], acc[2 * np][bq][3]),
                         cvtpk(acc[2 * np + 1][bq][0], acc[2 * np + 1][bq][1]), cvtpk(acc[2 * np + 1][bq][2], acc[2 * np + 1][bq][3])};
        __builtin_amdgcn_raw_buffer_store_b128(v, rt, lane * 16, WS_TILES_A + ((2 * idx + (bq >> 1)) * 8 + xcd) * TILE_B + ((cw * 2 + np) * 2 + (bq & 1)) * 1024, 16);
      }
    cbar_vm();
    if (cw == 0 && lane < 2) __hip_atomic_store((GAS u64*)(a.ws + WS_FLAGA + ((2 * idx + lane) * 8 + xcd) * 8), tag, RLX);
  } else {
    f32x4 oa[4][2];
#pragma unroll
    for (int nb = 0; nb < 4; ++nb) { oa[nb][0] = Z4; oa[nb][1] = Z4; }
    auto qproj = [&]() {
      f32x4 acc[2] = {Z4, Z4};
      const unsigned l = fresh(lane), c = l & 15, g = l >> 4, qq = c >> 2;
      const unsigned aq = L0 + (8 * g + qq) * 256 + (((unsigned)cw ^ (qq ^ (4 * (g & 1)))) << 5) + 8 * (l & 3);
      const unsigned ax0 = L0 + X_OFF + c * 1024 + ((g ^ c) << 4);
      u32x4 wf[2][2], xf[2][4];
      auto rd = [&](int st, int p) {
        const unsigned b = aq + sl, ax = ax0 ^ (((st + bB) & 7) << 7);
#pragma unroll
        for (int ks = 0; ks < 2; ++ks) wf[p][ks] = ldtr(b + ks * 8192, (b ^ 128) + 1024 + ks * 8192);
#pragma unroll
        for (int i = 0; i < 4; ++i) xf[p][i] = ld128((ax ^ ((i >> 1) << 6)) + (i & 1) * 16384);
        sl = nxth(sl);
        SB();
      };
      auto mm = [&](int p) {
#pragma unroll
        for (int i = 0; i < 4; ++i) acc[i & 1] = mfma(wf[p][i >> 1], xf[p][i], acc[i & 1]);
        SB();
      };
      rbar(); rd(0, 0);
#pragma unroll 1
      for (int st = 1; st < 7; st += 2) {
        rbar(); rd(st, 1); mm(0);
        rbar(); rd(st + 1, 0); mm(1);
      }
      rbar(); rd(7, 1); mm(0); mm(1);
#pragma unroll
      for (int qb = 0; qb < 2; ++qb) st64(AF() + cw * 512 + (Q_OFF - RING) + qb * 4096, cvtpk(acc[qb][0], acc[qb][1]), cvtpk(acc[qb][2], acc[qb][3]));
    };
    qproj();
    STAMP(8);
#pragma unroll 1
    for (int u = 0; u < nu; ++u) {
      f32x4 s[8];
      {
        u32x4 qf[4], kf[2][4];
        const unsigned aK = L0 + AK() + (cw >> 1) * 4096;
#pragma unroll
        for (int i = 0; i <= 8; ++i) {
          if (i < 8) {
            if (i == 0) cbar(); else rbar();
            const unsigned b = aK + sl;
#pragma unroll
            for (int kk = 0; kk < 4; ++kk) kf[i & 1][kk] = ld128(b ^ (kk << 6));
            if (i == 0) {
              const unsigned al = AL() + (Q_OFF - RING) + (cw & 1) * 4096;
#pragma unroll
              for (int kk = 0; kk < 4; ++kk) qf[kk] = ld128(al + kk * 1024);
            }
            sl = nxth(sl);
            SB();
          }
          if (i > 0) {
            s[i - 1] = Z4;
#pragma unroll
            for (int kk = 0; kk < 4; ++kk) s[i - 1] = mfma(kf[(i - 1) & 1][kk], qf[kk], s[i - 1]);
            SB();
          }
        }
      }
      if (u == 0) STAMP(1);
      const unsigned aS = L0 + ST_OFF + (fresh(lane) & 15) * 16;
      {
        float mx = s[0][0];
#pragma unroll
        for (int i = 0; i < 8; ++i)
#pragma unroll
          for (int r = 0; r < 4; ++r) mx = fmaxf(mx, s[i][r]);
        mx = xmax4(mx);
        if (lane < 16) *(LAS float*)(uintptr_t)(aS + (cw & 1) * 256 + (cw >> 1) * 4) = mx;
      }
      cbar();
      u32x2 pk[4];
      const unsigned oP = (P_OFF - RING) + (cw >> 2) * 2048 + (cw & 1) * 1024 + ((cw >> 1) & 1) * 8;
      {
        const unsigned aP = AL() + oP;
        const f32x4 m0 = ldf4(aS + (cw & 1) * 256);
        const float mn = SL2E * fmaxf(fmaxf(m0[0], m0[1]), fmaxf(m0[2], m0[3]));
        float ps = 0.f;
#pragma unroll
        for (int i = 0; i < 8; ++i) {
#pragma unroll
          for (int r = 0; r < 4; ++r) { s[i][r] = __builtin_amdgcn_exp2f(fmaf(s[i][r], SL2E, -mn)); ps += s[i][r]; }
          const unsigned lo = cvtpk(s[i][0], s[i][1]), hi = cvtpk(s[i][2], s[i][3]);
          if (i < 4) st64(aP + i * 4096, lo, hi); else pk[i - 4] = u32x2{lo, hi};
        }
        ps = xsum4(ps);
        if (lane < 16) *(LAS float*)(uintptr_t)(aS + 512 + (cw & 1) * 256 + (cw >> 1) * 4) = ps;
      }
      f32x4 o[2] = {Z4, Z4};
      auto vsteps = [&](int i0, int i1) {
        const unsigned aV = (L0 + AV()) ^ ((unsigned)cw << 5), ap = AL() + (P_OFF - RING);
#pragma unroll 1
        for (int i = i0; i < i1; ++i) {
          cbar();
          const unsigned b = aV + sl;
          u32x4 vf[2], pf[4];
#pragma unroll
          for (int ks = 0; ks < 2; ++ks) vf[ks] = ldtr(b + ks * 8192, b + ks * 8192 + 4096);
#pragma unroll
          for (int j = 0; j < 4; ++j) pf[j] = ld128(ap + (i & 3) * 4096 + j * 1024);
          sl = nxth(sl);
          if (i >= 1 && i <= 4) {
            const u32x2 v = i == 1 ? pk[0] : i == 2 ? pk[1] : i == 3 ? pk[2] : pk[3];
            st64(ap - (P_OFF - RING) + oP + (i - 1) * 4096, v[0], v[1]);
          }
          SB();
#pragma unroll
          for (int j = 0; j < 4; ++j) o[j & 1] = mfma(vf[j >> 1], pf[j], o[j & 1]);
        }
      };
      vsteps(0, pvB);
      float inv[2];
#pragma unroll
      for (int qb = 0; qb < 2; ++qb) { const f32x4 l0 = ldf4(aS + 512 + qb * 256); inv[qb] = 1.f / ((l0[0] + l0[1]) + (l0[2] + l0[3])); }
      if (u + 1 < nu) qproj();
      if (u == 1) STAMP(20);
      u32x4 wo[4][4];
      {
        const unsigned l = fresh(lane), g = l >> 4, qq = (l >> 2) & 3;
        const unsigned aw = L0 + (8 * g + qq) * 512 + ((((unsigned)cw * 2) ^ (qq ^ (4 * (g & 1)))) << 5) + 8 * (l & 3);
#pragma unroll
        for (int i = 0; i < 8; ++i) {
          SB(); asm volatile("s_waitcnt lgkmcnt(4)\n\ts_barrier" ::: "memory"); SB();
          const unsigned b = aw + sl;
#pragma unroll
          for (int nb = 0; nb < 2; ++nb) wo[i >> 1][(i & 1) * 2 + nb] = ldtr(b ^ (nb << 5), (b ^ (nb << 5) ^ 128) + 2048);
          sl = nxth(sl);
        }
      }
      vsteps(pvB, 8);
#pragma unroll
      for (int qb = 0; qb < 2; ++qb) {
        const f32x4 ov = o[qb] * inv[qb];
        st64(AF() + cw * 512 + (P_OFF - RING) + qb * 4096, cvtpk(ov[0], ov[1]), cvtpk(ov[2], ov[3]));
      }
      cbar();
      {
        u32x4 of[2][4];
        const unsigned al = AL() + (P_OFF - RING);
#pragma unroll
        for (int kk = 0; kk < 4; ++kk)
#pragma unroll
          for (int qb = 0; qb < 2; ++qb) of[qb][kk] = ld128(al + ((kk + bB) & 3) * 1024 + qb * 4096);
        SB();
#pragma unroll
        for (int kk = 0; kk < 4; ++kk)
#pragma unroll
          for (int qb = 0; qb < 2; ++qb)
#pragma unroll
            for (int nb = 0; nb < 4; ++nb) oa[nb][qb] = mfma(wo[kk][nb], of[qb][kk], oa[nb][qb]);
      }
      STAMP(2 + u);
    }
#pragma unroll
    for (int qb = 0; qb < 2; ++qb)
#pragma unroll
      for (int hf = 0; hf < 2; ++hf) {
        const u32x4 v = {cvtpk(oa[2 * hf][qb][0], oa[2 * hf][qb][1]), cvtpk(oa[2 * hf][qb][2], oa[2 * hf][qb][3]),
                         cvtpk(oa[2 * hf + 1][qb][0], oa[2 * hf + 1][qb][1]), cvtpk(oa[2 * hf + 1][qb][2], oa[2 * hf + 1][qb][3])};
        __builtin_amdgcn_raw_buffer_store_b128(v, rt, lane * 16, (bB * 12 + slotB) * TILE_B + ((hf * 8 + cw) * 2 + qb) * 1024, 16);
      }
    cbar_vm();
    if (cw == 0 && lane == 0) __hip_atomic_store((GAS u64*)(a.ws + WS_FLAGB + (bB * 16 + slotB) * 8), tag, RLX);
  }
  STAMP(5);

  {
    const unsigned c = lane & 15, g = lane >> 4;
    const int rb = wg >> 4, pc = (wg & 15) * 2 + ((cw >> 1) & 1), tg = cw & 1;
    const bool wB = cw < 4;
    const unsigned nt = wB ? 6 : 4;
    const unsigned fl = lane < nt ? lane : 0;
    const GAS u64* fp = (const GAS u64*)(a.ws + (wB ? WS_FLAGB + (rb * 16 + tg * 6 + fl) * 8 : WS_FLAGA + (rb * 8 + tg * 4 + fl) * 8));
#define NSPIN (1 << 16)
    for (int spin = 0; spin < NSPIN; ++spin) {
      if (__all(__hip_atomic_load(fp, RLX) == tag)) break;
      __builtin_amdgcn_s_sleep(4);
    }
    __builtin_amdgcn_fence(__ATOMIC_ACQUIRE, "wavefront");
    STAMP(6);
    const unsigned so = (wB ? (rb * 12 + tg * 6) * TILE_B : WS_TILES_A + (rb * 8 + tg * 4) * TILE_B) + pc * 1024;
    u32x4 t[6];
#pragma unroll
    for (int i = 0; i < 6; ++i) t[i] = __builtin_amdgcn_raw_buffer_load_b128(rt, (i < 4 || wB) ? lane * 16 : 0x7fffff00u, so + i * TILE_B, 16);
    const f32x4 s0 = ((bflo(t[0]) + bflo(t[1])) + (bflo(t[2]) + bflo(t[3]))) + (bflo(t[4]) + bflo(t[5]));
    const f32x4 s1 = ((bfhi(t[0]) + bfhi(t[1])) + (bfhi(t[2]) + bfhi(t[3]))) + (bfhi(t[4]) + bfhi(t[5]));
    *(LAS f32x4*)(uintptr_t)(L0 + cw * 1024 + lane * 16) = tg ? s0 : s1;
    cbar();
    const f32x4 oth = ldf4(L0 + (cw ^ 1) * 1024 + lane * 16);
    const f32x4 r = tg ? oth + s1 : s0 + oth;
    *(GAS u32x2*)(a.out + ((size_t)(rb * SQ + (pc & 1) * 16 + c) * 1024 + (wB ? 512 : 0) + 32 * (pc >> 1) + 16 * tg + 4 * g) * 2) = u32x2{cvtpk(r[0], r[1]), cvtpk(r[2], r[3])};
    if (wg == 255 && cw == 1) {
      for (int spin = 0; spin < NSPIN; ++spin) {
        bool ok = true;
#pragma unroll
        for (int i = 0; i < 4; ++i) ok &= (lane & 15) >= 12 || __hip_atomic_load((const GAS u64*)(a.ws + WS_FLAGB + (i * 64 + lane) * 8), RLX) == tag;
#pragma unroll
        for (int i = 0; i < 2; ++i) ok &= __hip_atomic_load((const GAS u64*)(a.ws + WS_FLAGA + (i * 64 + lane) * 8), RLX) == tag;
        if (__all(ok)) break;
        __builtin_amdgcn_s_sleep(2);
      }
      if (lane == 0) __hip_atomic_store((GAS u64*)a.ws, tag, RLX);
    }
  }
  STAMP(7);
}

extern "C" void kernel_launch(void* const* d_in, const int* in_sizes, int n_in, void* d_out, int out_size, void* d_ws, size_t ws_size,
                              hipStream_t stream) {
  Args a;
  for (int br = 0; br < 2; ++br) {
    a.x[br] = d_in[5 * br + 0];
    a.k[br] = d_in[5 * br + 1];
    a.v[br] = d_in[5 * br + 2];
    a.wq[br] = d_in[5 * br + 3];
    a.wo[br] = d_in[5 * br + 4];
  }
  a.ws = (char*)d_ws;
  a.out = (char*)d_out;
  (void)hipFuncSetAttribute((const void*)fused_k, hipFuncAttributeMaxDynamicSharedMemorySize, LDS_BYTES);
  fused_k<<<256, 768, LDS_BYTES, stream>>>(a);
}
```

```cpp
#include <hip/hip_runtime.h>
#include <stdint.h>

#define LAS __attribute__((address_space(3)))
#define GAS __attribute__((address_space(1)))
typedef unsigned u32x4 __attribute__((ext_vector_type(4)));
typedef unsigned u32x2 __attribute__((ext_vector_type(2)));
typedef float f32x4 __attribute__((ext_vector_type(4)));
typedef float f32x2 __attribute__((ext_vector_type(2)));
typedef short bf16x8 __attribute__((ext_vector_type(8)));
typedef short s16x4 __attribute__((ext_vector_type(4)));
typedef __bf16 bf16x2 __attribute__((ext_vector_type(2)));
typedef __amdgpu_buffer_rsrc_t rsrc_t;
typedef unsigned long long u64;

constexpr int NB = 16, SQ = 32, DM = 512, NH = 32, SK = 512, DH = 128;
constexpr float SL2E = 0.088388347648318447f * 1.4426950408889634f;
constexpr unsigned HT = 16384, RING = 6 * HT;
constexpr unsigned Q_OFF = 16384, ST_OFF = Q_OFF + 8192;
constexpr unsigned LDS_BYTES = 160 * 1024;
constexpr int NLD = 4;
constexpr unsigned KVB = SK * DH * 2;
constexpr unsigned WS_FLAG = 4096, WS_TILES = 1u << 20, TILE_B = 32768;
constexpr unsigned WS_TILES_A = 256 * TILE_B, WS_TILES_BYTES = 384 * TILE_B;

struct Args {
  const void* x[2]; const void* k[2]; const void* v[2]; const void* wq[2]; const void* wo[2];
  char* ws; char* out;
};

#define SB() __builtin_amdgcn_sched_barrier(0)
#define INL __attribute__((always_inline))
#define RLX __ATOMIC_RELAXED, __HIP_MEMORY_SCOPE_AGENT
#define Z4 f32x4{0.f, 0.f, 0.f, 0.f}
__device__ __forceinline__ rsrc_t mkrsrc(const void* p, unsigned bytes) { return __builtin_amdgcn_make_buffer_rsrc((void*)p, 0, bytes, 0x00020000); }
template <int AUX, int N> __device__ __forceinline__ void dma(rsrc_t r, unsigned lds, const unsigned (&v)[4], unsigned soff) {
  __builtin_amdgcn_raw_ptr_buffer_load_lds(r, (LAS void*)(uintptr_t)lds, 16, v[0], soff, 0, AUX);
  __builtin_amdgcn_raw_ptr_buffer_load_lds(r, (LAS void*)(uintptr_t)lds, 16, v[1], soff, 1024, AUX);
  if (N > 2) {
    __builtin_amdgcn_raw_ptr_buffer_load_lds(r, (LAS void*)(uintptr_t)lds, 16, v[2], soff, 2048, AUX);
    __builtin_amdgcn_raw_ptr_buffer_load_lds(r, (LAS void*)(uintptr_t)lds, 16, v[3], soff, 3072, AUX);
  }
}
template <int N> __device__ __forceinline__ void waitb() { asm volatile("s_waitcnt vmcnt(%0)\n\ts_barrier" :: "i"(N) : "memory"); }
#define WAITB(N) waitb<N>()
__device__ __forceinline__ void lbar() { asm volatile("s_barrier" ::: "memory"); }
__device__ __forceinline__ void rbar() { SB(); asm volatile("s_barrier" ::: "memory"); SB(); }
__device__ __forceinline__ void cbar() { SB(); asm volatile("s_waitcnt lgkmcnt(0)\n\ts_barrier" ::: "memory"); SB(); }
__device__ __forceinline__ void cbar_vm() { SB(); asm volatile("s_waitcnt vmcnt(0) lgkmcnt(0)\n\ts_barrier" ::: "memory"); SB(); }
__device__ __forceinline__ f32x4 mfma(u32x4 a, u32x4 b, f32x4 c) {
  return __builtin_amdgcn_mfma_f32_16x16x32_bf16(__builtin_bit_cast(bf16x8, a), __builtin_bit_cast(bf16x8, b), c, 0, 0, 0);
}
__device__ __forceinline__ unsigned cvtpk(float lo, float hi) { return __builtin_bit_cast(unsigned, __builtin_convertvector(f32x2{lo, hi}, bf16x2)); }
__device__ __forceinline__ u32x2 cvt4(f32x4 v) { return u32x2{cvtpk(v[0], v[1]), cvtpk(v[2], v[3])}; }
__device__ __forceinline__ u32x4 cvt8(f32x4 a, f32x4 b) { return u32x4{cvtpk(a[0], a[1]), cvtpk(a[2], a[3]), cvtpk(b[0], b[1]), cvtpk(b[2], b[3])}; }
__device__ __forceinline__ f32x4 bf4(unsigned lo, unsigned hi) { return f32x4{__uint_as_float(lo << 16), __uint_as_float(lo & 0xffff0000u), __uint_as_float(hi << 16), __uint_as_float(hi & 0xffff0000u)}; }
__device__ __forceinline__ u32x4 ld128(unsigned a) { return *(const LAS u32x4*)(uintptr_t)a; }
__device__ __forceinline__ f32x4 ldf4(unsigned a) { return *(const LAS f32x4*)(uintptr_t)a; }
__device__ __forceinline__ void st64(unsigned a, u32x2 v) { *(LAS u32x2*)(uintptr_t)a = v; }
__device__ __forceinline__ u32x4 ldtr(unsigned a0, unsigned a1) {
  const u32x2 lo = __builtin_bit_cast(u32x2, __builtin_amdgcn_ds_read_tr16_b64_v4i16((LAS s16x4*)(uintptr_t)a0));
  const u32x2 hi = __builtin_bit_cast(u32x2, __builtin_amdgcn_ds_read_tr16_b64_v4i16((LAS s16x4*)(uintptr_t)a1));
  return u32x4{lo[0], lo[1], hi[0], hi[1]};
}
__device__ __forceinline__ float xmax4(float x) {
  auto s = __builtin_amdgcn_permlane16_swap(__float_as_uint(x), __float_as_uint(x), false, false);
  x = fmaxf(__uint_as_float(s[0]), __uint_as_float(s[1]));
  auto t = __builtin_amdgcn_permlane32_swap(__float_as_uint(x), __float_as_uint(x), false, false);
  return fmaxf(__uint_as_float(t[0]), __uint_as_float(t[1]));
}
__device__ __forceinline__ float xsum4(float x) {
  auto s = __builtin_amdgcn_permlane16_swap(__float_as_uint(x), __float_as_uint(x), false, false);
  x = __uint_as_float(s[0]) + __uint_as_float(s[1]);
  auto t = __builtin_amdgcn_permlane32_swap(__float_as_uint(x), __float_as_uint(x), false, false);
  return __uint_as_float(t[0]) + __uint_as_float(t[1]);
}
__device__ __forceinline__ unsigned nxt(unsigned s) { s += HT; return s == RING ? 0u : s; }
__device__ __forceinline__ unsigned fresh(unsigned x) { asm volatile("" : "+v"(x)); return x; }
#define STAMP(i) do {} while (0)
#define KVMASK 0xffffffffu
#define KVAUX 2
#define EXP 2
#define THR 8.f
#define KROT 0
#define T1MAP
#define PVB 6
#define PACE2 0
#define PF 1
#define NSPIN (1 << 16)

#define TMARK(k) do {} while (0)
#define WGI ((b3 ? bat : afirst ? 16 + (bat >> 1) : 24 + (bat >> 1)) * 8 + xcd)

__global__ void __launch_bounds__(768) fused_k(Args a) {
  extern __shared__ __attribute__((aligned(16))) char smem[];
  const unsigned lane = threadIdx.x & 63;
  const int w = __builtin_amdgcn_readfirstlane(threadIdx.x >> 6);
  const int xcd = blockIdx.x & 7, pi = blockIdx.x >> 3;
  const int bat = pi >> 1;
  const bool b3 = !(pi & 1);
  const bool afirst = !b3 && !(bat & 1);
  const int nu = b3 ? 3 : 1;
  const int rotA = bat & 14;
  auto headB = [&](int u) INL { int x = u + bat % 3; x = x >= 3 ? x - 3 : x; return 4 * xcd + (b3 ? x : 3); };
  const unsigned xoffB = afirst ? 32768 : 0, poff = afirst ? 0 : 32768;
  const unsigned L0 = (unsigned)(uintptr_t)(LAS char*)smem;

  if (__builtin_expect(w < NLD, 1)) {
    const unsigned l = w;
    const unsigned s16 = lane & 15, e4 = lane >> 4, e2 = lane >> 5;
    auto offK = [&](int q, unsigned rs) INL { return (16 * l + e4) * rs + q * (4 * rs - 1024) + ((s16 ^ (4 * q + e4)) << 4); };
    auto offT = [&](int q, unsigned rs) INL { return (16 * l + e4) * rs + q * (4 * rs - 1024) + ((s16 ^ ((e4 ^ (((q ^ (q >> 1)) & 1) * 4)) << 1)) << 4); };
    auto offW = [&](int q, unsigned rs) INL { return (8 * l + e2) * rs + q * (2 * rs - 1024) + (((lane & 31) * 16) ^ (((2 * q + e2) ^ (4 * (l & 1))) * 32)); };
    const unsigned pl = L0 + l * 4096;
    unsigned sl = 0;
    auto wt = [&](rsrc_t r, const unsigned (&v)[4], unsigned so) INL { dma<0, 4>(r, pl + sl, v, so); sl = nxt(sl); };
    auto kvt = [&](rsrc_t r, const unsigned (&v)[4], unsigned so) INL { dma<2, 4>(r, pl + sl, v, so); sl = nxt(sl); };
    unsigned vX[4], vQ[4];
#pragma unroll
    for (int q = 0; q < 4; ++q) vX[q] = (8 * l + e4) * 1024 + q * 3072 + ((s16 ^ (8 * (l & 1) + 4 * q + e4)) << 4);
    auto xt = [&](rsrc_t rx, bool doA, int i) INL {
      const unsigned c4 = ((doA ? rotA >> 2 : bat >> 1) + i) & 3;
      dma<0, 2>(rx, L0 + RING + (doA ? 0 : xoffB) + c4 * 8192 + l * 2048, vX, (unsigned)bat * (SQ * DM * 2) + c4 * 256);
    };
    auto qoff = [&](bool doA, int hh, int i) INL { return doA ? ((rotA + (i >> 1)) & 15) * (32 * 8192) + xcd * 1024 + (i & 1) * 512 : ((i + bat) & 7) * (64 * 8192) + hh * 256; };
    int h = headB(0);
    {
      const rsrc_t rx = mkrsrc(afirst ? a.x[0] : a.x[1], NB * SQ * DM * 2), rq = mkrsrc(afirst ? a.wq[0] : a.wq[1], DM * NH * DH * 2);
      TMARK(126);
      xt(rx, afirst, 0);
#pragma unroll
      for (int q = 0; q < 4; ++q) vQ[q] = afirst ? offW(q, 8192) : offT(q, 8192);
      wt(rq, vQ, qoff(afirst, h, 0));
#pragma unroll
      for (int i = 1; i < 4; ++i) { xt(rx, afirst, i); wt(rq, vQ, qoff(afirst, h, i)); }
      TMARK(127);
      __builtin_amdgcn_s_setprio(3);
      WAITB(18); wt(rq, vQ, qoff(afirst, h, 4));
      WAITB(16); wt(rq, vQ, qoff(afirst, h, 5));
      WAITB(14); wt(rq, vQ, qoff(afirst, h, 6));
      WAITB(12); wt(rq, vQ, qoff(afirst, h, 7));
    }
    unsigned vK[4], vV[4], vO[4];
#pragma unroll
    for (int q = 0; q < 4; ++q) { vK[q] = offK(q, 256); vV[q] = offT(q, 256); vO[q] = offW(q, 1024); }
#pragma unroll 1
    for (int ph = 0; ph < (b3 ? 1 : 2); ++ph) {
      const bool doA = !b3 && (ph == 0) == afirst;
      const int br = doA ? 0 : 1;
      const unsigned kvbytes = (doA ? NB * 8 : NB * NH) * KVB;
      const rsrc_t rq = mkrsrc(a.wq[br], DM * NH * DH * 2);
      if (ph == 1) {
        const rsrc_t rx = mkrsrc(a.x[br], NB * SQ * DM * 2);
#pragma unroll
        for (int q = 0; q < 4; ++q) vQ[q] = doA ? offW(q, 8192) : offT(q, 8192);
#pragma unroll
        for (int i = 0; i < 8; ++i) {
          if (i >= 1 && i <= 3) WAITB(20); else WAITB(12);
          if (i == 3 && doA) lbar();
          if (i == 0) { xt(rx, doA, 0); xt(rx, doA, 1); xt(rx, doA, 2); xt(rx, doA, 3); }
          wt(rq, vQ, qoff(doA, h, i));
        }
      }
      const rsrc_t rk = mkrsrc(a.k[br], kvbytes), rv = mkrsrc(a.v[br], kvbytes), ro = mkrsrc(a.wo[br], DM * NH * DH * 2);
      if (doA) {
#pragma unroll 8
        for (int i = 8; i < 32; ++i) { WAITB(12); wt(rq, vQ, qoff(true, 0, i)); }
        const unsigned kvo = (unsigned)(bat * 8 + xcd) * KVB;
        WAITB(12); kvt(rk, vK, kvo);
#pragma unroll
        for (int j = 0; j < 7; ++j) {
          WAITB(12); if (j == 1) lbar();
          kvt(rk, vK, kvo + (j + 1) * HT);
          WAITB(12); kvt(rv, vV, kvo + j * HT);
        }
        WAITB(12); kvt(rv, vV, kvo + 7 * HT);
#pragma unroll 8
        for (int i = 0; i < 32; ++i) { WAITB(12); wt(ro, vO, xcd * (512 * 1024) + ((rotA + (i >> 1)) & 15) * (32 * 1024) + (i & 1) * 512); }
      } else {
        unsigned kvo = (unsigned)(bat * NH + h) * KVB;
#pragma unroll
        for (int i = 0; i < 4; ++i) { WAITB(12); kvt(rk, vK, kvo + i * HT); }
#pragma unroll 1
        for (int u = 0;; ++u) {
          const int hn = headB(u + 1);
#pragma unroll
          for (int i = 4; i < 8; ++i) { WAITB(12); kvt(rk, vK, kvo + i * HT); }
#pragma unroll
          for (int i = 0; i < PVB; ++i) { WAITB(12); if (i == 3) lbar(); kvt(rv, vV, kvo + i * HT); }
          if (u + 1 < nu) {
#pragma unroll
            for (int i = 0; i < 8; ++i) { WAITB(12); wt(rq, vQ, qoff(false, hn, i)); }
          }
#pragma unroll
          for (int i = 0; i < 8; ++i) { WAITB(12); wt(ro, vO, h * (128 * 1024) + (((i >> 1) + bat) & 3) * (32 * 1024) + (i & 1) * 512); }
#pragma unroll
          for (int i = PVB; i < 8; ++i) { WAITB(12); kvt(rv, vV, kvo + i * HT); }
          if (u + 1 == nu) break;
          h = hn;
          kvo = (unsigned)(bat * NH + h) * KVB;
#pragma unroll
          for (int i = 0; i < 4; ++i) { WAITB(12); if (i == 3) lbar(); kvt(rk, vK, kvo + i * HT); }
        }
      }
    }
    WAITB(12); WAITB(8); WAITB(4); WAITB(0);
    return;
  }

  const int cw = w - NLD;
  STAMP(0);
  const u64 ep = __hip_atomic_load((const GAS u64*)a.ws, RLX);
  const u64 tag = (((u64)(unsigned)__builtin_amdgcn_readfirstlane((unsigned)(ep >> 32)) << 32) | (unsigned)__builtin_amdgcn_readfirstlane((unsigned)ep)) + 0x9E3779B97F4A7C15ull;
  unsigned sl = 0;
  auto AK = [&]() INL { const unsigned l = fresh(lane), c = l & 15, g = l >> 4; return L0 + c * 256 + ((g ^ c) << 4); };
  auto AV = [&]() INL { const unsigned l = fresh(lane), g = l >> 4, qq = (l >> 2) & 3;
                    return L0 + (4 * g + qq) * 256 + (((4 * (g & 1) + qq) ^ (4 * (g >> 1))) << 5) + 8 * (l & 3); };
  auto AW = [&]() INL { const unsigned l = fresh(lane), g = l >> 4, qq = (l >> 2) & 3;
                    return L0 + (8 * g + qq) * 512 + ((((unsigned)cw * 2) ^ (qq ^ (4 * (g & 1)))) << 5) + 8 * (l & 3); };
  auto AF = [&]() INL { const unsigned l = fresh(lane); return L0 + RING + ((l & 15) + 16 * (l >> 5)) * 16 + 8 * ((l >> 4) & 1); };
  auto AL = [&]() INL { return L0 + RING + fresh(lane) * 16; };

  u32x4 tile[4];
#pragma unroll
  for (int i = 0; i < 4; ++i) tile[i] = u32x4{0u, 0u, 0u, 0u};
  auto stash = [&]() INL {
    const unsigned al = AL() + 32768 + cw * 4096;
#pragma unroll
    for (int i = 0; i < 4; ++i) *(LAS u32x4*)(uintptr_t)(al + i * 1024) = tile[i];
    SB();
  };
#define PHN ((b3 || afirst) ? 1 : 2)
#pragma unroll 1
  for (int ph = 0; ph < PHN; ++ph) {
   if (afirst || ph == 1) {
    f32x4 acc[4][2];
    auto gemm = [&](auto xaddr, unsigned xs, bool isq) INL {
#pragma unroll
      for (int i = 0; i < 4; ++i) { acc[i][0] = Z4; acc[i][1] = Z4; }
      u32x4 xf[2][2], wf[2][2];
      const unsigned aW = AW();
      auto step = [&](int ks, int hf, int xp, bool domm, bool dord) INL {
        const unsigned b = aW + sl, ax = xaddr((rotA + ks) & 15);
        const int phf = hf ^ 1, px = hf ? xp : xp ^ 1;
        if (dord) {
#pragma unroll
          for (int i = 0; i < 2; ++i) wf[hf][i] = ldtr(b ^ (i << 5), (b ^ (i << 5) ^ 128) + 2048);
          if (hf == 0) { xf[xp][0] = ld128(ax); xf[xp][1] = ld128(ax + xs); }
          sl = nxt(sl);
          SB();
        }
        if (domm) {
#pragma unroll
          for (int i = 0; i < 4; ++i) acc[phf * 2 + (i >> 1)][i & 1] = mfma(wf[phf][i >> 1], xf[px][i & 1], acc[phf * 2 + (i >> 1)][i & 1]);
          SB();
        }
      };
      cbar();
      step(0, 0, 0, false, true);
      if (isq && ph == 1) stash();
      rbar(); step(0, 1, 0, true, true);
#pragma unroll 1
      for (int ks = 1; ks < 15; ks += 2) {
        rbar(); step(ks, 0, 1, true, true);
        rbar(); step(ks, 1, 1, true, true);
        rbar(); step(ks + 1, 0, 0, true, true);
        rbar(); step(ks + 1, 1, 0, true, true);
      }
      rbar(); step(15, 0, 1, true, true);
      rbar(); step(15, 1, 1, true, true);
      if (isq) rbar();
      step(0, 0, 0, true, false);
    };
    const unsigned aK = AK(), aV = AV();
    gemm([&](unsigned ks) INL { return (aK + RING + (ks >> 2) * 8192) ^ ((ks & 3) << 6); }, 4096, true);
    STAMP(9);
    {
      const unsigned aF = AF() + ((cw >> 2) * 8 + (cw & 3)) * 1024;
#pragma unroll
      for (int i = 0; i < 4; ++i)
#pragma unroll
        for (int qb = 0; qb < 2; ++qb) st64(aF + (i >> 1) * 16384 + qb * 4096 + (i & 1) * 512, cvt4(acc[i][qb]));
    }
    {
      constexpr int PD = 6;
      u32x4 fr[8], qf[4], pf[2];
      f32x4 o[8], sa[4], sb[4], ls = Z4;
      auto rdK = [&](unsigned b, int i) INL { fr[i & 7] = ld128((b ^ ((i & 3) << 6)) + (i >> 2) * 4096); };
      auto rdV = [&](unsigned b, int i) INL { const unsigned ad = (b ^ ((i & 7) << 5)) + (i >> 3) * 8192; fr[i & 7] = ldtr(ad, ad + 4096); };
      auto one = [](float x) INL { asm volatile("" : "+v"(x)); return x; };
      float m = -INFINITY, mx;
      const u32x4 ones = {0x3f803f80u, 0x3f803f80u, 0x3f803f80u, 0x3f803f80u};
      auto phaseA = [&](f32x4 (&sn)[4], f32x4 (&sc)[4], bool domm, bool dosm, bool nextK) INL {
        const unsigned bc = sl + aK;
        if (domm) sl = nxt(sl);
        const unsigned bn = sl + (nextK ? aK : aV);
#pragma unroll
        for (int i = 0; i < 16; ++i) {
          if (domm) {
            if (i == 16 - PD) rbar();
            sn[i >> 2] = mfma(fr[i & 7], qf[i & 3], (i & 3) ? sn[i >> 2] : Z4);
            if (i + PD < 16) rdK(bc, i + PD); else if (nextK) rdK(bn, i + PD - 16); else rdV(bn, i + PD - 16);
          }
          if (dosm) {
            sc[i >> 2][i & 3] = __builtin_amdgcn_exp2f(one(fmaf(sc[i >> 2][i & 3], SL2E, -m)));
            if (i & 1) pf[i >> 3][(i >> 1) & 3] = cvtpk(sc[i >> 2][(i & 3) - 1], sc[i >> 2][i & 3]);
          }
          if (i & 1) SB();
        }
      };
      auto phaseB = [&](f32x4 (&sn)[4], bool bar, bool nextK) INL {
        const unsigned bc = sl + aV;
        sl = nxt(sl);
        const unsigned bn = sl + (nextK ? aK : aV);
#pragma unroll
        for (int i = 0; i < 16; ++i) {
          if (i == 16 - PD && bar) rbar();
          o[i & 7] = mfma(fr[i & 7], pf[i >> 3], o[i & 7]);
          if (i + PD < 16) rdV(bc, i + PD); else if (!bar) {} else if (nextK) rdK(bn, i + PD - 16); else rdV(bn, i + PD - 16);
          if ((i & 7) == 3) ls = mfma(ones, pf[i >> 3], ls);
          if (i & 1) { if (bar) mx = fmaxf(fmaxf(sn[i >> 2][(i & 3) - 1], sn[i >> 2][i & 3]), i == 1 ? -INFINITY : mx); SB(); }
        }
      };
      auto check = [&]() INL {
        if (__any(!(mx * SL2E - m <= THR))) {
          const float mn = fmaxf(m, xmax4(mx) * SL2E);
          const float alpha = __builtin_amdgcn_exp2f(m - mn);
          m = mn; ls *= alpha;
#pragma unroll
          for (int i = 0; i < 8; ++i) o[i] *= alpha;
        }
        SB();
      };
#pragma unroll
      for (int i = 0; i < 8; ++i) o[i] = Z4;
      cbar();
      {
        const unsigned aq = AL() + cw * 4096;
#pragma unroll
        for (int kk = 0; kk < 4; ++kk) qf[kk] = ld128(aq + kk * 1024);
      }
#pragma unroll
      for (int i = 0; i < PD; ++i) rdK(aK + sl, i);
      phaseA(sa, sb, true, false, true);
      m = sa[0][0];
#pragma unroll
      for (int i = 0; i < 4; ++i) m = fmaxf(fmaxf(fmaxf(sa[i][0], sa[i][1]), m), fmaxf(sa[i][2], sa[i][3]));
      m = xmax4(m) * SL2E;
#pragma unroll 1
      for (int j = 0; j < 6; j += 2) {
        phaseA(sb, sa, true, true, false); phaseB(sb, true, true); check();
        phaseA(sa, sb, true, true, false); phaseB(sa, true, true); check();
      }
      phaseA(sb, sa, true, true, false); phaseB(sb, true, false); check();
      phaseA(sa, sb, false, true, false); phaseB(sa, false, false);
      const float inv = __builtin_amdgcn_rcpf(ls[0]);
      const unsigned aF = AF() + ((cw & 1) * 16 + (cw >> 1) * 4) * 1024;
#pragma unroll
      for (int db = 0; db < 8; ++db) st64(aF + db * 512, cvt4(o[db] * inv));
    }
    STAMP(10);
    {
      const unsigned al = AL();
      gemm([&](unsigned ks) INL { return al + ks * 1024; }, 16384, false);
    }
#pragma unroll
    for (int i = 0; i < 4; ++i) tile[i] = cvt8(acc[i & 2][i & 1], acc[(i & 2) + 1][i & 1]);
    STAMP(11);
   }
   if (ph == 0) {
    unsigned aq, ax0;
    {
      const unsigned l = fresh(lane), g = l >> 4, qq = (l >> 2) & 3;
      aq = L0 + (8 * g + qq) * 256 + (((unsigned)cw ^ (qq ^ (4 * (g & 1)))) << 5) + 8 * (l & 3);
      ax0 = AK();
    }
    const unsigned al = AL() + poff, aF = AF() + cw * 512 + poff;
    f32x4 qa[2];
    u32x4 wf[2][2], xf[2][4];
    auto qrd = [&](int st, int p) INL {
      const unsigned ii = (st + bat) & 7, b = aq + sl, ax = (ax0 + (RING + xoffB + (ii >> 1) * 8192)) ^ ((ii & 1) << 7);
#pragma unroll
      for (int ks = 0; ks < 2; ++ks) wf[p][ks] = ldtr(b + ks * 8192, (b ^ 128) + 1024 + ks * 8192);
#pragma unroll
      for (int i = 0; i < 4; ++i) xf[p][i] = ld128((ax ^ ((i >> 1) << 6)) + (i & 1) * 4096);
      sl = nxt(sl);
      SB();
    };
    auto qmm = [&](int p) INL {
#pragma unroll
      for (int i = 0; i < 4; ++i) qa[i & 1] = mfma(wf[p][i >> 1], xf[p][i], qa[i & 1]);
      SB();
    };
    auto qrest = [&]() INL {
#pragma unroll 1
      for (int st = 1; st < 7; st += 2) {
        rbar(); qrd(st, 1); qmm(0);
        rbar(); qrd(st + 1, 0); qmm(1);
      }
      rbar(); qrd(7, 1); qmm(0);
    };
    auto qwr = [&]() INL {
#pragma unroll
      for (int qb = 0; qb < 2; ++qb) st64(aF + Q_OFF + qb * 4096, cvt4(qa[qb]));
      SB();
    };
    qa[0] = Z4; qa[1] = Z4;
    rbar(); qrd(0, 0); qrest(); qmm(1); qwr();
    STAMP(8);
    cbar();
    if (afirst) stash();
    f32x4 oa[4][2];
#pragma unroll
    for (int nb = 0; nb < 4; ++nb) { oa[nb][0] = Z4; oa[nb][1] = Z4; }
#pragma unroll 1
    for (int u = 0; u < nu; ++u) {
      f32x4 s[8];
      {
        u32x4 qf[4], kf[2][4];
        const unsigned aK = ax0 + (cw >> 1) * 4096;
#pragma unroll
        for (int i = 0; i <= 8; ++i) {
          if (i < 8) {
            if (i > 0 || u > 0) rbar();
            const unsigned b = aK + sl;
#pragma unroll
            for (int kk = 0; kk < 4; ++kk) kf[i & 1][kk] = ld128(b ^ (kk << 6));
            if (i == 0) {
#pragma unroll
              for (int kk = 0; kk < 4; ++kk) qf[kk] = ld128(al + (Q_OFF + (cw & 1) * 4096) + kk * 1024);
            }
            sl = nxt(sl);
            SB();
          }
          if (i > 0) {
            s[i - 1] = Z4;
#pragma unroll
            for (int kk = 0; kk < 4; ++kk) s[i - 1] = mfma(kf[(i - 1) & 1][kk], qf[kk], s[i - 1]);
            SB();
          }
        }
      }
      if (u == 0) STAMP(1);
      const unsigned aS = L0 + RING + poff + ST_OFF + (fresh(lane) & 15) * 16;
      {
        float mx = s[0][0];
#pragma unroll
        for (int i = 0; i < 8; ++i)
#pragma unroll
          for (int r = 0; r < 4; ++r) mx = fmaxf(mx, s[i][r]);
        mx = xmax4(mx);
        if (lane < 16) *(LAS float*)(uintptr_t)(aS + (cw & 1) * 256 + (cw >> 1) * 4) = mx;
      }
      cbar();
      u32x2 pk[4];
      const unsigned aP = al + ((cw >> 2) * 2048 + (cw & 1) * 1024 + ((cw >> 1) & 1) * 8);
      {
        const f32x4 m0 = ldf4(aS + (cw & 1) * 256);
        const float mn = SL2E * fmaxf(fmaxf(m0[0], m0[1]), fmaxf(m0[2], m0[3]));
        f32x4 ps = Z4;
#pragma unroll
        for (int i = 0; i < 8; ++i) {
          const f32x4 e = s[i] * SL2E - mn;
#pragma unroll
          for (int r = 0; r < 4; ++r) s[i][r] = __builtin_amdgcn_exp2f(e[r]);
          ps += s[i];
          if (i < 4) st64(aP + i * 4096, cvt4(s[i])); else pk[i - 4] = cvt4(s[i]);
        }
        const float pt = xsum4((ps[0] + ps[1]) + (ps[2] + ps[3]));
        if (lane < 16) *(LAS float*)(uintptr_t)(aS + 512 + (cw & 1) * 256 + (cw >> 1) * 4) = pt;
      }
      f32x4 o[2] = {Z4, Z4};
      float inv[2];
      const unsigned aV = AV() ^ ((unsigned)cw << 5), aw = AW();
      u32x4 vf[2][2], pf[2][4], wo[4][4];
      auto vrd = [&](int i) INL {
        if (i >= 2 && i < 6) st64(aP + (i - 2) * 4096, pk[i - 2]);
        SB();
        const unsigned b = aV + sl;
#pragma unroll
        for (int ks = 0; ks < 2; ++ks) vf[i & 1][ks] = ldtr(b + ks * 8192, b + ks * 8192 + 4096);
#pragma unroll
        for (int j = 0; j < 4; ++j) pf[i & 1][j] = ld128(al + ((i & 3) * 4096 + j * 1024));
        sl = nxt(sl);
        SB();
      };
      auto vmm = [&](int i) INL {
#pragma unroll
        for (int j = 0; j < 4; ++j) o[j & 1] = mfma(vf[i & 1][j >> 1], pf[i & 1][j], o[j & 1]);
        SB();
      };
      auto wrd = [&](int i) INL {
        const unsigned b = aw + sl;
#pragma unroll
        for (int nb = 0; nb < 2; ++nb) wo[i >> 1][(i & 1) * 2 + nb] = ldtr(b ^ (nb << 5), (b ^ (nb << 5) ^ 128) + 2048);
        sl = nxt(sl);
        SB();
      };
#pragma unroll
      for (int i = 0; i < PVB; ++i) {
        if (i == 0) cbar(); else rbar();
        vrd(i);
        if (i > 0) vmm(i - 1);
        if (i == 3) {
#pragma unroll
          for (int qb = 0; qb < 2; ++qb) { const f32x4 l0 = ldf4(aS + 512 + qb * 256); inv[qb] = __builtin_amdgcn_rcpf((l0[0] + l0[1]) + (l0[2] + l0[3])); }
          SB();
        }
      }
      if (u + 1 < nu) {
        qa[0] = Z4; qa[1] = Z4;
        rbar(); qrd(0, 0); vmm(PVB - 1);
        qrest();
        rbar(); wrd(0); qmm(1);
        rbar(); wrd(1); qwr();
      } else {
        rbar(); wrd(0); vmm(PVB - 1);
        rbar(); wrd(1);
      }
#pragma unroll
      for (int i = 2; i < 8; ++i) {
        SB(); asm volatile("s_waitcnt lgkmcnt(4)\n\ts_barrier" ::: "memory"); SB();
        wrd(i);
      }
#pragma unroll
      for (int i = PVB; i < 8; ++i) {
        if (i == PVB) cbar(); else rbar();
        vrd(i); vmm(i);
      }
#pragma unroll
      for (int qb = 0; qb < 2; ++qb) st64(aF + qb * 4096, cvt4(o[qb] * inv[qb]));
      cbar();
      {
        u32x4 of[2][4];
#pragma unroll
        for (int qb = 0; qb < 2; ++qb)
#pragma unroll
          for (int kk = 0; kk < 4; ++kk) of[qb][kk] = ld128(al + ((kk + bat) & 3) * 1024 + qb * 4096);
        SB();
#pragma unroll
        for (int qb = 0; qb < 2; ++qb)
#pragma unroll
          for (int kk = 0; kk < 4; ++kk)
#pragma unroll
            for (int nb = 0; nb < 4; ++nb) oa[nb][qb] = mfma(wo[kk][nb], of[qb][kk], oa[nb][qb]);
      }
      STAMP(2 + u);
    }
#pragma unroll
    for (int i = 0; i < 4; ++i) tile[i] = cvt8(oa[i & 2][i & 1], oa[(i & 2) + 1][i & 1]);
   }
  }

  const rsrc_t rt = mkrsrc(a.ws + WS_TILES, WS_TILES_BYTES);
  {
    const unsigned tB = (bat * 16 + (b3 ? 0 : 8) + xcd) * TILE_B, tA = WS_TILES_A + (bat * 8 + xcd) * TILE_B;
    const unsigned tl = (b3 || afirst) ? tB : tA;
    if (!b3) {
      const unsigned al = AL() + 32768 + cw * 4096;
#pragma unroll
      for (int i = 0; i < 4; ++i) __builtin_amdgcn_raw_buffer_store_b128(ld128(al + i * 1024), rt, lane * 16, (tA + tB - tl) + (((i >> 1) * 8 + cw) * 2 + (i & 1)) * 1024, 16);
    }
#pragma unroll
    for (int i = 0; i < 4; ++i) __builtin_amdgcn_raw_buffer_store_b128(tile[i], rt, lane * 16, tl + (((i >> 1) * 8 + cw) * 2 + (i & 1)) * 1024, 16);
    asm volatile("s_waitcnt vmcnt(0)" ::: "memory");
    if (lane == 0) __hip_atomic_store((GAS u64*)(a.ws + WS_FLAG + ((bat * 8 + cw) * 16 + (b3 ? 0 : 8) + xcd) * 8), tag, RLX);
  }
  STAMP(5);

  {
    const int rb = blockIdx.x >> 4, pj = blockIdx.x & 15, pc = pj * 2 + ((cw >> 1) & 1), hb = cw & 1;
    const bool wB = cw < 4;
    const GAS u64* fp = (const GAS u64*)(a.ws + WS_FLAG + ((rb * 8 + (pj & 7)) * 16 + (wB ? (lane & 15) : 8 + (lane & 7))) * 8);
    for (int spin = 0; spin < NSPIN; ++spin) {
      if (__all(__hip_atomic_load(fp, RLX) == tag)) break;
      __builtin_amdgcn_s_sleep(2);
    }
    __builtin_amdgcn_fence(__ATOMIC_ACQUIRE, "wavefront");
    STAMP(6);
    const unsigned so = (wB ? rb * 16 * TILE_B : WS_TILES_A + rb * 8 * TILE_B) + pc * 1024 + hb * 512;
    const unsigned vo = (lane >> 5) * TILE_B + (lane & 31) * 16;
    u32x4 t[8];
#pragma unroll
    for (int i = 0; i < 8; ++i) t[i] = __builtin_amdgcn_raw_buffer_load_b128(rt, (i < 4 || wB) ? vo : 0x7fffff00u, so + i * 2 * TILE_B, 16);
    f32x4 s0 = Z4, s1 = Z4;
#pragma unroll
    for (int i = 0; i < 8; ++i) { s0 += bf4(t[i][0], t[i][1]); s1 += bf4(t[i][2], t[i][3]); }
    f32x4 r;
#pragma unroll
    for (int j = 0; j < 4; ++j) {
      const auto x = __builtin_amdgcn_permlane32_swap(__float_as_uint(s0[j]), __float_as_uint(s1[j]), false, false);
      r[j] = __uint_as_float(x[0]) + __uint_as_float(x[1]);
    }
    const unsigned sl_ = hb * 32 + (lane & 31), c = sl_ & 15, g = sl_ >> 4;
    *(GAS u32x2*)(a.out + ((size_t)(rb * SQ + (pc & 1) * 16 + c) * 1024 + (wB ? 512 : 0) + 32 * (pc >> 1) + 16 * (lane >> 5) + 4 * g) * 2) = cvt4(r);
    if (blockIdx.x == 255 && cw == 1) {
      for (int spin = 0; spin < NSPIN; ++spin) {
        bool ok = true;
#pragma unroll
        for (int i = 0; i < 4; ++i) ok &= __hip_atomic_load((const GAS u64*)(a.ws + WS_FLAG + ((i * 4 + (lane >> 4)) * 128 + (lane & 15)) * 8), RLX) == tag;
        if (__all(ok)) break;
        __builtin_amdgcn_s_sleep(2);
      }
      if (lane == 0) __hip_atomic_store((GAS u64*)a.ws, tag, RLX);
    }
  }
  STAMP(7);
}

extern "C" void kernel_launch(void* const* d_in, const int* in_sizes, int n_in, void* d_out, int out_size, void* d_ws, size_t ws_size,
                              hipStream_t stream) {
  Args a;
  for (int br = 0; br < 2; ++br) {
    a.x[br] = d_in[5 * br + 0];
    a.k[br] = d_in[5 * br + 1];
    a.v[br] = d_in[5 * br + 2];
    a.wq[br] = d_in[5 * br + 3];
    a.wo[br] = d_in[5 * br + 4];
  }
  a.ws = (char*)d_ws;
  a.out = (char*)d_out;
  (void)hipFuncSetAttribute((const void*)fused_k, hipFuncAttributeMaxDynamicSharedMemorySize, LDS_BYTES);
  fused_k<<<256, 768, LDS_BYTES, stream>>>(a);
}
```

```cpp
#define STAMPS
#include <hip/hip_runtime.h>
#include <stdint.h>

#define LAS __attribute__((address_space(3)))
#define GAS __attribute__((address_space(1)))
typedef unsigned u32x4 __attribute__((ext_vector_type(4)));
typedef unsigned u32x2 __attribute__((ext_vector_type(2)));
typedef float f32x4 __attribute__((ext_vector_type(4)));
typedef float f32x2 __attribute__((ext_vector_type(2)));
typedef short bf16x8 __attribute__((ext_vector_type(8)));
typedef short s16x4 __attribute__((ext_vector_type(4)));
typedef __bf16 bf16x2 __attribute__((ext_vector_type(2)));
typedef __amdgpu_buffer_rsrc_t rsrc_t;
typedef unsigned long long u64;

constexpr int NB = 16, SQ = 32, DM = 512, NH = 32, SK = 512, DH = 128;
constexpr float SL2E = 0.088388347648318447f * 1.4426950408889634f;
constexpr unsigned HT = 16384, RING = 6 * HT;
constexpr unsigned Q_OFF = 16384, ST_OFF = Q_OFF + 8192;
constexpr unsigned LDS_BYTES = 160 * 1024;
constexpr int NLD = 4;
constexpr unsigned KVB = SK * DH * 2;
constexpr unsigned WS_FLAG = 4096, WS_TILES = 1u << 20, TILE_B = 32768;
constexpr unsigned WS_TILES_A = 256 * TILE_B, WS_TILES_BYTES = 384 * TILE_B;

struct Args {
  const void* x[2]; const void* k[2]; const void* v[2]; const void* wq[2]; const void* wo[2];
  char* ws; char* out;
  u64* stamps;
};

#define SB() __builtin_amdgcn_sched_barrier(0)
#define INL __attribute__((always_inline))
#define RLX __ATOMIC_RELAXED, __HIP_MEMORY_SCOPE_AGENT
#define Z4 f32x4{0.f, 0.f, 0.f, 0.f}
__device__ __forceinline__ rsrc_t mkrsrc(const void* p, unsigned bytes) { return __builtin_amdgcn_make_buffer_rsrc((void*)p, 0, bytes, 0x00020000); }
template <int AUX, int N> __device__ __forceinline__ void dma(rsrc_t r, unsigned lds, const unsigned (&v)[4], unsigned soff) {
  __builtin_amdgcn_raw_ptr_buffer_load_lds(r, (LAS void*)(uintptr_t)lds, 16, v[0], soff, 0, AUX);
  __builtin_amdgcn_raw_ptr_buffer_load_lds(r, (LAS void*)(uintptr_t)lds, 16, v[1], soff, 1024, AUX);
  if (N > 2) {
    __builtin_amdgcn_raw_ptr_buffer_load_lds(r, (LAS void*)(uintptr_t)lds, 16, v[2], soff, 2048, AUX);
    __builtin_amdgcn_raw_ptr_buffer_load_lds(r, (LAS void*)(uintptr_t)lds, 16, v[3], soff, 3072, AUX);
  }
}
template <int N> __device__ __forceinline__ void waitb() { asm volatile("s_waitcnt vmcnt(%0)\n\ts_barrier" :: "i"(N) : "memory"); }
#define WAITB(N) waitb<N>()
__device__ __forceinline__ void lbar() { asm volatile("s_barrier" ::: "memory"); }
__device__ __forceinline__ void rbar() { SB(); asm volatile("s_barrier" ::: "memory"); SB(); }
__device__ __forceinline__ void cbar() { SB(); asm volatile("s_waitcnt lgkmcnt(0)\n\ts_barrier" ::: "memory"); SB(); }
__device__ __forceinline__ void cbar_vm() { SB(); asm volatile("s_waitcnt vmcnt(0) lgkmcnt(0)\n\ts_barrier" ::: "memory"); SB(); }
__device__ __forceinline__ f32x4 mfma(u32x4 a, u32x4 b, f32x4 c) {
  return __builtin_amdgcn_mfma_f32_16x16x32_bf16(__builtin_bit_cast(bf16x8, a), __builtin_bit_cast(bf16x8, b), c, 0, 0, 0);
}
__device__ __forceinline__ unsigned cvtpk(float lo, float hi) { return __builtin_bit_cast(unsigned, __builtin_convertvector(f32x2{lo, hi}, bf16x2)); }
__device__ __forceinline__ u32x2 cvt4(f32x4 v) { return u32x2{cvtpk(v[0], v[1]), cvtpk(v[2], v[3])}; }
__device__ __forceinline__ u32x4 cvt8(f32x4 a, f32x4 b) { return u32x4{cvtpk(a[0], a[1]), cvtpk(a[2], a[3]), cvtpk(b[0], b[1]), cvtpk(b[2], b[3])}; }
__device__ __forceinline__ f32x4 bf4(unsigned lo, unsigned hi) { return f32x4{__uint_as_float(lo << 16), __uint_as_float(lo & 0xffff0000u), __uint_as_float(hi << 16), __uint_as_float(hi & 0xffff0000u)}; }
__device__ __forceinline__ u32x4 ld128(unsigned a) { return *(const LAS u32x4*)(uintptr_t)a; }
__device__ __forceinline__ f32x4 ldf4(unsigned a) { return *(const LAS f32x4*)(uintptr_t)a; }
__device__ __forceinline__ void st64(unsigned a, u32x2 v) { *(LAS u32x2*)(uintptr_t)a = v; }
__device__ __forceinline__ u32x4 ldtr(unsigned a0, unsigned a1) {
  const u32x2 lo = __builtin_bit_cast(u32x2, __builtin_amdgcn_ds_read_tr16_b64_v4i16((LAS s16x4*)(uintptr_t)a0));
  const u32x2 hi = __builtin_bit_cast(u32x2, __builtin_amdgcn_ds_read_tr16_b64_v4i16((LAS s16x4*)(uintptr_t)a1));
  return u32x4{lo[0], lo[1], hi[0], hi[1]};
}
__device__ __forceinline__ float xmax4(float x) {
  auto s = __builtin_amdgcn_permlane16_swap(__float_as_uint(x), __float_as_uint(x), false, false);
  x = fmaxf(__uint_as_float(s[0]), __uint_as_float(s[1]));
  auto t = __builtin_amdgcn_permlane32_swap(__float_as_uint(x), __float_as_uint(x), false, false);
  return fmaxf(__uint_as_float(t[0]), __uint_as_float(t[1]));
}
__device__ __forceinline__ float xsum4(float x) {
  auto s = __builtin_amdgcn_permlane16_swap(__float_as_uint(x), __float_as_uint(x), false, false);
  x = __uint_as_float(s[0]) + __uint_as_float(s[1]);
  auto t = __builtin_amdgcn_permlane32_swap(__float_as_uint(x), __float_as_uint(x), false, false);
  return __uint_as_float(t[0]) + __uint_as_float(t[1]);
}
__device__ __forceinline__ unsigned nxt(unsigned s) { s += HT; return s == RING ? 0u : s; }
__device__ __forceinline__ unsigned fresh(unsigned x) { asm volatile("" : "+v"(x)); return x; }
#define STAMP(i) do { unsigned t_ = (unsigned)__builtin_amdgcn_s_memrealtime(); _Pragma("unroll") for (int k_ = 0; k_ < 12; ++k_) if (k_ == (i)) stv[k_] = t_; } while (0)
#define KVMASK 0xffffffffu
#define KVAUX 2
#define EXP 2
#define THR 8.f
#define KROT 0
#define T1MAP
#define PVB 6
#define PACE2 0
#define PF 1
#define NSPIN (1 << 16)

#define TMARK(k) do {} while (0)
#define WGI ((b3 ? bat : afirst ? 16 + (bat >> 1) : 24 + (bat >> 1)) * 8 + xcd)

__global__ void __launch_bounds__(768) fused_k(Args a) {
  extern __shared__ __attribute__((aligned(16))) char smem[];
  const unsigned lane = threadIdx.x & 63;
  const int w = __builtin_amdgcn_readfirstlane(threadIdx.x >> 6);
  const int xcd = blockIdx.x & 7, pi = blockIdx.x >> 3;
  const int bat = pi >> 1;
  const bool b3 = !(pi & 1);
  const bool afirst = !b3 && !(bat & 1);
  const int nu = b3 ? 3 : 1;
  const int rotA = bat & 14;
  auto headB = [&](int u) INL { int x = u + bat % 3; x = x >= 3 ? x - 3 : x; return 4 * xcd + (b3 ? x : 3); };
  const unsigned xoffB = afirst ? 32768 : 0, poff = afirst ? 0 : 32768;
  const unsigned L0 = (unsigned)(uintptr_t)(LAS char*)smem;

  if (__builtin_expect(w < NLD, 1)) {
    const unsigned l = w;
    const unsigned s16 = lane & 15, e4 = lane >> 4, e2 = lane >> 5;
    auto offK = [&](int q, unsigned rs) INL { return (16 * l + e4) * rs + q * (4 * rs - 1024) + ((s16 ^ (4 * q + e4)) << 4); };
    auto offT = [&](int q, unsigned rs) INL { return (16 * l + e4) * rs + q * (4 * rs - 1024) + ((s16 ^ ((e4 ^ (((q ^ (q >> 1)) & 1) * 4)) << 1)) << 4); };
    auto offW = [&](int q, unsigned rs) INL { return (8 * l + e2) * rs + q * (2 * rs - 1024) + (((lane & 31) * 16) ^ (((2 * q + e2) ^ (4 * (l & 1))) * 32)); };
    const unsigned pl = L0 + l * 4096;
    unsigned sl = 0;
    auto wt = [&](rsrc_t r, const unsigned (&v)[4], unsigned so) INL { dma<0, 4>(r, pl + sl, v, so); sl = nxt(sl); };
    auto kvt = [&](rsrc_t r, const unsigned (&v)[4], unsigned so) INL { dma<2, 4>(r, pl + sl, v, so); sl = nxt(sl); };
    unsigned vX[4], vQ[4];
#pragma unroll
    for (int q = 0; q < 4; ++q) vX[q] = (8 * l + e4) * 1024 + q * 3072 + ((s16 ^ (8 * (l & 1) + 4 * q + e4)) << 4);
    auto xt = [&](rsrc_t rx, bool doA, int i) INL {
      const unsigned c4 = ((doA ? rotA >> 2 : bat >> 1) + i) & 3;
      dma<0, 2>(rx, L0 + RING + (doA ? 0 : xoffB) + c4 * 8192 + l * 2048, vX, (unsigned)bat * (SQ * DM * 2) + c4 * 256);
    };
    auto qoff = [&](bool doA, int hh, int i) INL { return doA ? ((rotA + (i >> 1)) & 15) * (32 * 8192) + xcd * 1024 + (i & 1) * 512 : ((i + bat) & 7) * (64 * 8192) + hh * 256; };
    int h = headB(0);
    {
      const rsrc_t rx = mkrsrc(afirst ? a.x[0] : a.x[1], NB * SQ * DM * 2), rq = mkrsrc(afirst ? a.wq[0] : a.wq[1], DM * NH * DH * 2);
      TMARK(126);
      xt(rx, afirst, 0);
#pragma unroll
      for (int q = 0; q < 4; ++q) vQ[q] = afirst ? offW(q, 8192) : offT(q, 8192);
      wt(rq, vQ, qoff(afirst, h, 0));
#pragma unroll
      for (int i = 1; i < 4; ++i) { xt(rx, afirst, i); wt(rq, vQ, qoff(afirst, h, i)); }
      TMARK(127);
      __builtin_amdgcn_s_setprio(3);
      WAITB(18); wt(rq, vQ, qoff(afirst, h, 4));
      WAITB(16); wt(rq, vQ, qoff(afirst, h, 5));
      WAITB(14); wt(rq, vQ, qoff(afirst, h, 6));
      WAITB(12); wt(rq, vQ, qoff(afirst, h, 7));
    }
    unsigned vK[4], vV[4], vO[4];
#pragma unroll
    for (int q = 0; q < 4; ++q) { vK[q] = offK(q, 256); vV[q] = offT(q, 256); vO[q] = offW(q, 1024); }
#pragma unroll 1
    for (int ph = 0; ph < (b3 ? 1 : 2); ++ph) {
      const bool doA = !b3 && (ph == 0) == afirst;
      const int br = doA ? 0 : 1;
      const unsigned kvbytes = (doA ? NB * 8 : NB * NH) * KVB;
      const rsrc_t rq = mkrsrc(a.wq[br], DM * NH * DH * 2);
      if (ph == 1) {
        const rsrc_t rx = mkrsrc(a.x[br], NB * SQ * DM * 2);
#pragma unroll
        for (int q = 0; q < 4; ++q) vQ[q] = doA ? offW(q, 8192) : offT(q, 8192);
#pragma unroll
        for (int i = 0; i < 8; ++i) {
          if (i >= 1 && i <= 3) WAITB(20); else WAITB(12);
          if (i == 3 && doA) lbar();
          if (i == 0) { xt(rx, doA, 0); xt(rx, doA, 1); xt(rx, doA, 2); xt(rx, doA, 3); }
          wt(rq, vQ, qoff(doA, h, i));
        }
      }
      const rsrc_t rk = mkrsrc(a.k[br], kvbytes), rv = mkrsrc(a.v[br], kvbytes), ro = mkrsrc(a.wo[br], DM * NH * DH * 2);
      if (doA) {
#pragma unroll 8
        for (int i = 8; i < 32; ++i) { WAITB(12); wt(rq, vQ, qoff(true, 0, i)); }
        const unsigned kvo = (unsigned)(bat * 8 + xcd) * KVB;
        WAITB(12); kvt(rk, vK, kvo);
#pragma unroll
        for (int j = 0; j < 7; ++j) {
          WAITB(12); if (j == 1) lbar();
          kvt(rk, vK, kvo + (j + 1) * HT);
          WAITB(12); kvt(rv, vV, kvo + j * HT);
        }
        WAITB(12); kvt(rv, vV, kvo + 7 * HT);
#pragma unroll 8
        for (int i = 0; i < 32; ++i) { WAITB(12); wt(ro, vO, xcd * (512 * 1024) + ((rotA + (i >> 1)) & 15) * (32 * 1024) + (i & 1) * 512); }
      } else {
        unsigned kvo = (unsigned)(bat * NH + h) * KVB;
#pragma unroll
        for (int i = 0; i < 4; ++i) { WAITB(12); kvt(rk, vK, kvo + i * HT); }
#pragma unroll 1
        for (int u = 0;; ++u) {
          const int hn = headB(u + 1);
#pragma unroll
          for (int i = 4; i < 8; ++i) { WAITB(12); kvt(rk, vK, kvo + i * HT); }
#pragma unroll
          for (int i = 0; i < PVB; ++i) { WAITB(12); if (i == 3) lbar(); kvt(rv, vV, kvo + i * HT); }
          if (u + 1 < nu) {
#pragma unroll
            for (int i = 0; i < 8; ++i) { WAITB(12); wt(rq, vQ, qoff(false, hn, i)); }
          }
#pragma unroll
          for (int i = 0; i < 8; ++i) { WAITB(12); wt(ro, vO, h * (128 * 1024) + (((i >> 1) + bat) & 3) * (32 * 1024) + (i & 1) * 512); }
#pragma unroll
          for (int i = PVB; i < 8; ++i) { WAITB(12); kvt(rv, vV, kvo + i * HT); }
          if (u + 1 == nu) break;
          h = hn;
          kvo = (unsigned)(bat * NH + h) * KVB;
#pragma unroll
          for (int i = 0; i < 4; ++i) { WAITB(12); if (i == 3) lbar(); kvt(rk, vK, kvo + i * HT); }
        }
      }
    }
    WAITB(12); WAITB(8); WAITB(4); WAITB(0);
    return;
  }

  const int cw = w - NLD;
  unsigned stv[12];
#pragma unroll
  for (int k_ = 0; k_ < 12; ++k_) stv[k_] = 0;
  const u64 clk0 = __builtin_amdgcn_s_memtime();
  STAMP(0);
  const u64 ep = __hip_atomic_load((const GAS u64*)a.ws, RLX);
  const u64 tag = (((u64)(unsigned)__builtin_amdgcn_readfirstlane((unsigned)(ep >> 32)) << 32) | (unsigned)__builtin_amdgcn_readfirstlane((unsigned)ep)) + 0x9E3779B97F4A7C15ull;
  unsigned sl = 0;
  auto AK = [&]() INL { const unsigned l = fresh(lane), c = l & 15, g = l >> 4; return L0 + c * 256 + ((g ^ c) << 4); };
  auto AV = [&]() INL { const unsigned l = fresh(lane), g = l >> 4, qq = (l >> 2) & 3;
                    return L0 + (4 * g + qq) * 256 + (((4 * (g & 1) + qq) ^ (4 * (g >> 1))) << 5) + 8 * (l & 3); };
  auto AW = [&]() INL { const unsigned l = fresh(lane), g = l >> 4, qq = (l >> 2) & 3;
                    return L0 + (8 * g + qq) * 512 + ((((unsigned)cw * 2) ^ (qq ^ (4 * (g & 1)))) << 5) + 8 * (l & 3); };
  auto AF = [&]() INL { const unsigned l = fresh(lane); return L0 + RING + ((l & 15) + 16 * (l >> 5)) * 16 + 8 * ((l >> 4) & 1); };
  auto AL = [&]() INL { return L0 + RING + fresh(lane) * 16; };

  u32x4 tile[4];
#pragma unroll
  for (int i = 0; i < 4; ++i) tile[i] = u32x4{0u, 0u, 0u, 0u};
  auto stash = [&]() INL {
    const unsigned al = AL() + 32768 + cw * 4096;
#pragma unroll
    for (int i = 0; i < 4; ++i) *(LAS u32x4*)(uintptr_t)(al + i * 1024) = tile[i];
    SB();
  };
#define PHN ((b3 || afirst) ? 1 : 2)
#pragma unroll 1
  for (int ph = 0; ph < PHN; ++ph) {
   if (afirst || ph == 1) {
    f32x4 acc[4][2];
    auto gemm = [&](auto xaddr, unsigned xs, bool isq) INL {
#pragma unroll
      for (int i = 0; i < 4; ++i) { acc[i][0] = Z4; acc[i][1] = Z4; }
      u32x4 xf[2][2], wf[2][2];
      const unsigned aW = AW();
      auto step = [&](int ks, int hf, int xp, bool domm, bool dord) INL {
        const unsigned b = aW + sl, ax = xaddr((rotA + ks) & 15);
        const int phf = hf ^ 1, px = hf ? xp : xp ^ 1;
        if (dord) {
#pragma unroll
          for (int i = 0; i < 2; ++i) wf[hf][i] = ldtr(b ^ (i << 5), (b ^ (i << 5) ^ 128) + 2048);
          if (hf == 0) { xf[xp][0] = ld128(ax); xf[xp][1] = ld128(ax + xs); }
          sl = nxt(sl);
          SB();
        }
        if (domm) {
#pragma unroll
          for (int i = 0; i < 4; ++i) acc[phf * 2 + (i >> 1)][i & 1] = mfma(wf[phf][i >> 1], xf[px][i & 1], acc[phf * 2 + (i >> 1)][i & 1]);
          SB();
        }
      };
      cbar();
      step(0, 0, 0, false, true);
      if (isq && ph == 1) stash();
      rbar(); step(0, 1, 0, true, true);
#pragma unroll 1
      for (int ks = 1; ks < 15; ks += 2) {
        rbar(); step(ks, 0, 1, true, true);
        rbar(); step(ks, 1, 1, true, true);
        rbar(); step(ks + 1, 0, 0, true, true);
        rbar(); step(ks + 1, 1, 0, true, true);
      }
      rbar(); step(15, 0, 1, true, true);
      rbar(); step(15, 1, 1, true, true);
      if (isq) rbar();
      step(0, 0, 0, true, false);
    };
    const unsigned aK = AK(), aV = AV();
    gemm([&](unsigned ks) INL { return (aK + RING + (ks >> 2) * 8192) ^ ((ks & 3) << 6); }, 4096, true);
    STAMP(9);
    {
      const unsigned aF = AF() + ((cw >> 2) * 8 + (cw & 3)) * 1024;
#pragma unroll
      for (int i = 0; i < 4; ++i)
#pragma unroll
        for (int qb = 0; qb < 2; ++qb) st64(aF + (i >> 1) * 16384 + qb * 4096 + (i & 1) * 512, cvt4(acc[i][qb]));
    }
    {
      constexpr int PD = 6;
      u32x4 fr[8], qf[4], pf[2];
      f32x4 o[8], sa[4], sb[4], ls = Z4;
      auto rdK = [&](unsigned b, int i) INL { fr[i & 7] = ld128((b ^ ((i & 3) << 6)) + (i >> 2) * 4096); };
      auto rdV = [&](unsigned b, int i) INL { const unsigned ad = (b ^ ((i & 7) << 5)) + (i >> 3) * 8192; fr[i & 7] = ldtr(ad, ad + 4096); };
      auto one = [](float x) INL { asm volatile("" : "+v"(x)); return x; };
      float m = -INFINITY, mx;
      const u32x4 ones = {0x3f803f80u, 0x3f803f80u, 0x3f803f80u, 0x3f803f80u};
      auto phaseA = [&](f32x4 (&sn)[4], f32x4 (&sc)[4], bool domm, bool dosm, bool nextK) INL {
        const unsigned bc = sl + aK;
        if (domm) sl = nxt(sl);
        const unsigned bn = sl + (nextK ? aK : aV);
#pragma unroll
        for (int i = 0; i < 16; ++i) {
          if (domm) {
            if (i == 16 - PD) rbar();
            sn[i >> 2] = mfma(fr[i & 7], qf[i & 3], (i & 3) ? sn[i >> 2] : Z4);
            if (i + PD < 16) rdK(bc, i + PD); else if (nextK) rdK(bn, i + PD - 16); else rdV(bn, i + PD - 16);
          }
          if (dosm) {
            sc[i >> 2][i & 3] = __builtin_amdgcn_exp2f(one(fmaf(sc[i >> 2][i & 3], SL2E, -m)));
            if (i & 1) pf[i >> 3][(i >> 1) & 3] = cvtpk(sc[i >> 2][(i & 3) - 1], sc[i >> 2][i & 3]);
          }
          if (i & 1) SB();
        }
      };
      auto phaseB = [&](f32x4 (&sn)[4], bool bar, bool nextK) INL {
        const unsigned bc = sl + aV;
        sl = nxt(sl);
        const unsigned bn = sl + (nextK ? aK : aV);
#pragma unroll
        for (int i = 0; i < 16; ++i) {
          if (i == 16 - PD && bar) rbar();
          o[i & 7] = mfma(fr[i & 7], pf[i >> 3], o[i & 7]);
          if (i + PD < 16) rdV(bc, i + PD); else if (!bar) {} else if (nextK) rdK(bn, i + PD - 16); else rdV(bn, i + PD - 16);
          if ((i & 7) == 3) ls = mfma(ones, pf[i >> 3], ls);
          if (i & 1) { if (bar) mx = fmaxf(fmaxf(sn[i >> 2][(i & 3) - 1], sn[i >> 2][i & 3]), i == 1 ? -INFINITY : mx); SB(); }
        }
      };
      auto check = [&]() INL {
        if (__any(!(mx * SL2E - m <= THR))) {
          const float mn = fmaxf(m, xmax4(mx) * SL2E);
          const float alpha = __builtin_amdgcn_exp2f(m - mn);
          m = mn; ls *= alpha;
#pragma unroll
          for (int i = 0; i < 8; ++i) o[i] *= alpha;
        }
        SB();
      };
#pragma unroll
      for (int i = 0; i < 8; ++i) o[i] = Z4;
      cbar();
      {
        const unsigned aq = AL() + cw * 4096;
#pragma unroll
        for (int kk = 0; kk < 4; ++kk) qf[kk] = ld128(aq + kk * 1024);
      }
#pragma unroll
      for (int i = 0; i < PD; ++i) rdK(aK + sl, i);
      phaseA(sa, sb, true, false, true);
      m = sa[0][0];
#pragma unroll
      for (int i = 0; i < 4; ++i) m = fmaxf(fmaxf(fmaxf(sa[i][0], sa[i][1]), m), fmaxf(sa[i][2], sa[i][3]));
      m = xmax4(m) * SL2E;
#pragma unroll 1
      for (int j = 0; j < 6; j += 2) {
        phaseA(sb, sa, true, true, false); phaseB(sb, true, true); check();
        phaseA(sa, sb, true, true, false); phaseB(sa, true, true); check();
      }
      phaseA(sb, sa, true, true, false); phaseB(sb, true, false); check();
      phaseA(sa, sb, false, true, false); phaseB(sa, false, false);
      const float inv = __builtin_amdgcn_rcpf(ls[0]);
      const unsigned aF = AF() + ((cw & 1) * 16 + (cw >> 1) * 4) * 1024;
#pragma unroll
      for (int db = 0; db < 8; ++db) st64(aF + db * 512, cvt4(o[db] * inv));
    }
    STAMP(10);
    {
      const unsigned al = AL();
      gemm([&](unsigned ks) INL { return al + ks * 1024; }, 16384, false);
    }
#pragma unroll
    for (int i = 0; i < 4; ++i) tile[i] = cvt8(acc[i & 2][i & 1], acc[(i & 2) + 1][i & 1]);
    STAMP(11);
   }
   if (ph == 0) {
    unsigned aq, ax0;
    {
      const unsigned l = fresh(lane), g = l >> 4, qq = (l >> 2) & 3;
      aq = L0 + (8 * g + qq) * 256 + (((unsigned)cw ^ (qq ^ (4 * (g & 1)))) << 5) + 8 * (l & 3);
      ax0 = AK();
    }
    const unsigned al = AL() + poff, aF = AF() + cw * 512 + poff;
    f32x4 qa[2];
    u32x4 wf[2][2], xf[2][4];
    auto qrd = [&](int st, int p) INL {
      const unsigned ii = (st + bat) & 7, b = aq + sl, ax = (ax0 + (RING + xoffB + (ii >> 1) * 8192)) ^ ((ii & 1) << 7);
#pragma unroll
      for (int ks = 0; ks < 2; ++ks) wf[p][ks] = ldtr(b + ks * 8192, (b ^ 128) + 1024 + ks * 8192);
#pragma unroll
      for (int i = 0; i < 4; ++i) xf[p][i] = ld128((ax ^ ((i >> 1) << 6)) + (i & 1) * 4096);
      sl = nxt(sl);
      SB();
    };
    auto qmm = [&](int p) INL {
#pragma unroll
      for (int i = 0; i < 4; ++i) qa[i & 1] = mfma(wf[p][i >> 1], xf[p][i], qa[i & 1]);
      SB();
    };
    auto qrest = [&]() INL {
#pragma unroll 1
      for (int st = 1; st < 7; st += 2) {
        rbar(); qrd(st, 1); qmm(0);
        rbar(); qrd(st + 1, 0); qmm(1);
      }
      rbar(); qrd(7, 1); qmm(0);
    };
    auto qwr = [&]() INL {
#pragma unroll
      for (int qb = 0; qb < 2; ++qb) st64(aF + Q_OFF + qb * 4096, cvt4(qa[qb]));
      SB();
    };
    qa[0] = Z4; qa[1] = Z4;
    rbar(); qrd(0, 0); qrest(); qmm(1); qwr();
    STAMP(8);
    cbar();
    if (afirst) stash();
    f32x4 oa[4][2];
#pragma unroll
    for (int nb = 0; nb < 4; ++nb) { oa[nb][0] = Z4; oa[nb][1] = Z4; }
#pragma unroll 1
    for (int u = 0; u < nu; ++u) {
      f32x4 s[8];
      {
        u32x4 qf[4], kf[2][4];
        const unsigned aK = ax0 + (cw >> 1) * 4096;
#pragma unroll
        for (int i = 0; i <= 8; ++i) {
          if (i < 8) {
            if (i > 0 || u > 0) rbar();
            const unsigned b = aK + sl;
#pragma unroll
            for (int kk = 0; kk < 4; ++kk) kf[i & 1][kk] = ld128(b ^ (kk << 6));
            if (i == 0) {
#pragma unroll
              for (int kk = 0; kk < 4; ++kk) qf[kk] = ld128(al + (Q_OFF + (cw & 1) * 4096) + kk * 1024);
            }
            sl = nxt(sl);
            SB();
          }
          if (i > 0) {
            s[i - 1] = Z4;
#pragma unroll
            for (int kk = 0; kk < 4; ++kk) s[i - 1] = mfma(kf[(i - 1) & 1][kk], qf[kk], s[i - 1]);
            SB();
          }
        }
      }
      if (u == 0) STAMP(1);
      const unsigned aS = L0 + RING + poff + ST_OFF + (fresh(lane) & 15) * 16;
      {
        float mx = s[0][0];
#pragma unroll
        for (int i = 0; i < 8; ++i)
#pragma unroll
          for (int r = 0; r < 4; ++r) mx = fmaxf(mx, s[i][r]);
        mx = xmax4(mx);
        if (lane < 16) *(LAS float*)(uintptr_t)(aS + (cw & 1) * 256 + (cw >> 1) * 4) = mx;
      }
      cbar();
      u32x2 pk[4];
      const unsigned aP = al + ((cw >> 2) * 2048 + (cw & 1) * 1024 + ((cw >> 1) & 1) * 8);
      {
        const f32x4 m0 = ldf4(aS + (cw & 1) * 256);
        const float mn = SL2E * fmaxf(fmaxf(m0[0], m0[1]), fmaxf(m0[2], m0[3]));
        f32x4 ps = Z4;
#pragma unroll
        for (int i = 0; i < 8; ++i) {
          const f32x4 e = s[i] * SL2E - mn;
#pragma unroll
          for (int r = 0; r < 4; ++r) s[i][r] = __builtin_amdgcn_exp2f(e[r]);
          ps += s[i];
          if (i < 4) st64(aP + i * 4096, cvt4(s[i])); else pk[i - 4] = cvt4(s[i]);
        }
        const float pt = xsum4((ps[0] + ps[1]) + (ps[2] + ps[3]));
        if (lane < 16) *(LAS float*)(uintptr_t)(aS + 512 + (cw & 1) * 256 + (cw >> 1) * 4) = pt;
      }
      f32x4 o[2] = {Z4, Z4};
      float inv[2];
      const unsigned aV = AV() ^ ((unsigned)cw << 5), aw = AW();
      u32x4 vf[2][2], pf[2][4], wo[4][4];
      auto vrd = [&](int i) INL {
        if (i >= 2 && i < 6) st64(aP + (i - 2) * 4096, pk[i - 2]);
        SB();
        const unsigned b = aV + sl;
#pragma unroll
        for (int ks = 0; ks < 2; ++ks) vf[i & 1][ks] = ldtr(b + ks * 8192, b + ks * 8192 + 4096);
#pragma unroll
        for (int j = 0; j < 4; ++j) pf[i & 1][j] = ld128(al + ((i & 3) * 4096 + j * 1024));
        sl = nxt(sl);
        SB();
      };
      auto vmm = [&](int i) INL {
#pragma unroll
        for (int j = 0; j < 4; ++j) o[j & 1] = mfma(vf[i & 1][j >> 1], pf[i & 1][j], o[j & 1]);
        SB();
      };
      auto wrd = [&](int i) INL {
        const unsigned b = aw + sl;
#pragma unroll
        for (int nb = 0; nb < 2; ++nb) wo[i >> 1][(i & 1) * 2 + nb] = ldtr(b ^ (nb << 5), (b ^ (nb << 5) ^ 128) + 2048);
        sl = nxt(sl);
        SB();
      };
#pragma unroll
      for (int i = 0; i < PVB; ++i) {
        if (i == 0) cbar(); else rbar();
        vrd(i);
        if (i > 0) vmm(i - 1);
        if (i == 3) {
#pragma unroll
          for (int qb = 0; qb < 2; ++qb) { const f32x4 l0 = ldf4(aS + 512 + qb * 256); inv[qb] = __builtin_amdgcn_rcpf((l0[0] + l0[1]) + (l0[2] + l0[3])); }
          SB();
        }
      }
      if (u + 1 < nu) {
        qa[0] = Z4; qa[1] = Z4;
        rbar(); qrd(0, 0); vmm(PVB - 1);
        qrest();
        rbar(); wrd(0); qmm(1);
        rbar(); wrd(1); qwr();
      } else {
        rbar(); wrd(0); vmm(PVB - 1);
        rbar(); wrd(1);
      }
#pragma unroll
      for (int i = 2; i < 8; ++i) {
        SB(); asm volatile("s_waitcnt lgkmcnt(4)\n\ts_barrier" ::: "memory"); SB();
        wrd(i);
      }
#pragma unroll
      for (int i = PVB; i < 8; ++i) {
        if (i == PVB) cbar(); else rbar();
        vrd(i); vmm(i);
      }
#pragma unroll
      for (int qb = 0; qb < 2; ++qb) st64(aF + qb * 4096, cvt4(o[qb] * inv[qb]));
      cbar();
      {
        u32x4 of[2][4];
#pragma unroll
        for (int qb = 0; qb < 2; ++qb)
#pragma unroll
          for (int kk = 0; kk < 4; ++kk) of[qb][kk] = ld128(al + ((kk + bat) & 3) * 1024 + qb * 4096);
        SB();
#pragma unroll
        for (int qb = 0; qb < 2; ++qb)
#pragma unroll
          for (int kk = 0; kk < 4; ++kk)
#pragma unroll
            for (int nb = 0; nb < 4; ++nb) oa[nb][qb] = mfma(wo[kk][nb], of[qb][kk], oa[nb][qb]);
      }
      STAMP(2 + u);
    }
#pragma unroll
    for (int i = 0; i < 4; ++i) tile[i] = cvt8(oa[i & 2][i & 1], oa[(i & 2) + 1][i & 1]);
   }
  }

  const rsrc_t rt = mkrsrc(a.ws + WS_TILES, WS_TILES_BYTES);
  {
    const unsigned tB = (bat * 16 + (b3 ? 0 : 8) + xcd) * TILE_B, tA = WS_TILES_A + (bat * 8 + xcd) * TILE_B;
    const unsigned tl = (b3 || afirst) ? tB : tA;
    if (!b3) {
      const unsigned al = AL() + 32768 + cw * 4096;
#pragma unroll
      for (int i = 0; i < 4; ++i) __builtin_amdgcn_raw_buffer_store_b128(ld128(al + i * 1024), rt, lane * 16, (tA + tB - tl) + (((i >> 1) * 8 + cw) * 2 + (i & 1)) * 1024, 16);
    }
#pragma unroll
    for (int i = 0; i < 4; ++i) __builtin_amdgcn_raw_buffer_store_b128(tile[i], rt, lane * 16, tl + (((i >> 1) * 8 + cw) * 2 + (i & 1)) * 1024, 16);
    asm volatile("s_waitcnt vmcnt(0)" ::: "memory");
    if (lane == 0) __hip_atomic_store((GAS u64*)(a.ws + WS_FLAG + ((bat * 8 + cw) * 16 + (b3 ? 0 : 8) + xcd) * 8), tag, RLX);
  }
  STAMP(5);

  {
    const int rb = blockIdx.x >> 4, pj = blockIdx.x & 15, pc = pj * 2 + ((cw >> 1) & 1), hb = cw & 1;
    const bool wB = cw < 4;
    const GAS u64* fp = (const GAS u64*)(a.ws + WS_FLAG + ((rb * 8 + (pj & 7)) * 16 + (wB ? (lane & 15) : 8 + (lane & 7))) * 8);
    for (int spin = 0; spin < NSPIN; ++spin) {
      if (__all(__hip_atomic_load(fp, RLX) == tag)) break;
      __builtin_amdgcn_s_sleep(2);
    }
    __builtin_amdgcn_fence(__ATOMIC_ACQUIRE, "wavefront");
    STAMP(6);
    const unsigned so = (wB ? rb * 16 * TILE_B : WS_TILES_A + rb * 8 * TILE_B) + pc * 1024 + hb * 512;
    const unsigned vo = (lane >> 5) * TILE_B + (lane & 31) * 16;
    u32x4 t[8];
#pragma unroll
    for (int i = 0; i < 8; ++i) t[i] = __builtin_amdgcn_raw_buffer_load_b128(rt, (i < 4 || wB) ? vo : 0x7fffff00u, so + i * 2 * TILE_B, 16);
    f32x4 s0 = Z4, s1 = Z4;
#pragma unroll
    for (int i = 0; i < 8; ++i) { s0 += bf4(t[i][0], t[i][1]); s1 += bf4(t[i][2], t[i][3]); }
    f32x4 r;
#pragma unroll
    for (int j = 0; j < 4; ++j) {
      const auto x = __builtin_amdgcn_permlane32_swap(__float_as_uint(s0[j]), __float_as_uint(s1[j]), false, false);
      r[j] = __uint_as_float(x[0]) + __uint_as_float(x[1]);
    }
    const unsigned sl_ = hb * 32 + (lane & 31), c = sl_ & 15, g = sl_ >> 4;
    *(GAS u32x2*)(a.out + ((size_t)(rb * SQ + (pc & 1) * 16 + c) * 1024 + (wB ? 512 : 0) + 32 * (pc >> 1) + 16 * (lane >> 5) + 4 * g) * 2) = cvt4(r);
    if (blockIdx.x == 255 && cw == 1) {
      for (int spin = 0; spin < NSPIN; ++spin) {
        bool ok = true;
#pragma unroll
        for (int i = 0; i < 4; ++i) ok &= __hip_atomic_load((const GAS u64*)(a.ws + WS_FLAG + ((i * 4 + (lane >> 4)) * 128 + (lane & 15)) * 8), RLX) == tag;
        if (__all(ok)) break;
        __builtin_amdgcn_s_sleep(2);
      }
      if (lane == 0) __hip_atomic_store((GAS u64*)a.ws, tag, RLX);
    }
  }
  STAMP(7);
  if (cw == 0 && lane == 0) {
    a.stamps[WGI * 32 + 31] = __builtin_amdgcn_s_memtime() - clk0;
#pragma unroll
    for (int k_ = 0; k_ < 12; ++k_) a.stamps[WGI * 32 + k_] = stv[k_];
  }
}

__global__ void spin_k(const u64* st) {
  __shared__ unsigned red[2][256];
  const int i = threadIdx.x;
  red[0][i] = (unsigned)st[i * 32]; red[1][i] = (unsigned)st[i * 32 + 8];
  __syncthreads();
  if (i == 0) {
    unsigned mn = red[0][0];
    for (int j = 0; j < 256; ++j) mn = (int)(red[0][j] - mn) < 0 ? red[0][j] : mn;
    unsigned v = 0;
    if (0) { for (int j = 0; j < 256; ++j) v = max(v, red[1][j] - mn); }
    else { for (int j = 0; j < 128; ++j) v += red[1][j] - mn; v /= 128; }
    if (v > 10000) v = 10000;
    const u64 s0 = __builtin_amdgcn_s_memrealtime();
    while (__builtin_amdgcn_s_memrealtime() - s0 < 20ull * v) __builtin_amdgcn_s_sleep(4);
  }
}
extern "C" void kernel_launch(void* const* d_in, const int* in_sizes, int n_in, void* d_out, int out_size, void* d_ws, size_t ws_size,
                              hipStream_t stream) {
  Args a;
  for (int br = 0; br < 2; ++br) {
    a.x[br] = d_in[5 * br + 0];
    a.k[br] = d_in[5 * br + 1];
    a.v[br] = d_in[5 * br + 2];
    a.wq[br] = d_in[5 * br + 3];
    a.wo[br] = d_in[5 * br + 4];
  }
  a.ws = (char*)d_ws;
  a.out = (char*)d_out;
  a.stamps = (u64*)((char*)d_ws + (64u << 20));
  (void)hipFuncSetAttribute((const void*)fused_k, hipFuncAttributeMaxDynamicSharedMemorySize, LDS_BYTES);
  fused_k<<<256, 768, LDS_BYTES, stream>>>(a);
  spin_k<<<1, 256, 0, stream>>>(a.stamps);
}
```

```cpp
#include <hip/hip_runtime.h>
#include <stdint.h>

#define LAS __attribute__((address_space(3)))
#define GAS __attribute__((address_space(1)))
typedef unsigned u32x4 __attribute__((ext_vector_type(4)));
typedef unsigned u32x2 __attribute__((ext_vector_type(2)));
typedef float f32x4 __attribute__((ext_vector_type(4)));
typedef float f32x2 __attribute__((ext_vector_type(2)));
typedef short bf16x8 __attribute__((ext_vector_type(8)));
typedef short s16x4 __attribute__((ext_vector_type(4)));
typedef __bf16 bf16x2 __attribute__((ext_vector_type(2)));
typedef __amdgpu_buffer_rsrc_t rsrc_t;
typedef unsigned long long u64;

constexpr int NB = 16, SQ = 32, DM = 512, NH = 32, SK = 512, DH = 128;
constexpr float SL2E = 0.088388347648318447f * 1.4426950408889634f;
constexpr unsigned HT = 16384, RING = 6 * HT;
constexpr unsigned X_OFF = 0, P_OFF = 32768, Q_OFF = P_OFF + 16384, ST_OFF = Q_OFF + 8192;
constexpr unsigned LDS_BYTES = 160 * 1024;
constexpr int NLD = 4;
constexpr unsigned KVB = SK * DH * 2;
constexpr unsigned WS_FLAGB = 4096, WS_FLAGA = 8192, WS_TILES = 1u << 20, TILE_B = 32768;
constexpr unsigned WS_TILES_A = 192 * TILE_B, WS_TILES_BYTES = 320 * TILE_B;

struct Args {
  const void* x[2]; const void* k[2]; const void* v[2]; const void* wq[2]; const void* wo[2];
  char* ws; char* out;
};

#define SB() __builtin_amdgcn_sched_barrier(0)
#define RLX __ATOMIC_RELAXED, __HIP_MEMORY_SCOPE_AGENT
#define Z4 f32x4{0.f, 0.f, 0.f, 0.f}
__device__ __forceinline__ rsrc_t mkrsrc(const void* p, unsigned bytes) { return __builtin_amdgcn_make_buffer_rsrc((void*)p, 0, bytes, 0x00020000); }
template <int AUX, int N> __device__ __forceinline__ void dma(rsrc_t r, unsigned lds, const unsigned (&v)[4], unsigned soff) {
  __builtin_amdgcn_raw_ptr_buffer_load_lds(r, (LAS void*)(uintptr_t)lds, 16, v[0], soff, 0, AUX);
  __builtin_amdgcn_raw_ptr_buffer_load_lds(r, (LAS void*)(uintptr_t)lds, 16, v[1], soff, 1024, AUX);
  if (N > 2) {
    __builtin_amdgcn_raw_ptr_buffer_load_lds(r, (LAS void*)(uintptr_t)lds, 16, v[2], soff, 2048, AUX);
    __builtin_amdgcn_raw_ptr_buffer_load_lds(r, (LAS void*)(uintptr_t)lds, 16, v[3], soff, 3072, AUX);
  }
}
template <int N> __device__ __forceinline__ void waitb() { asm volatile("s_waitcnt vmcnt(%0)\n\ts_barrier" :: "i"(N) : "memory"); }
#define WAITB(N) waitb<N>()
__device__ __forceinline__ void lbar() { asm volatile("s_barrier" ::: "memory"); }
__device__ __forceinline__ void rbar() { SB(); asm volatile("s_barrier" ::: "memory"); SB(); }
__device__ __forceinline__ void cbar() { SB(); asm volatile("s_waitcnt lgkmcnt(0)\n\ts_barrier" ::: "memory"); SB(); }
__device__ __forceinline__ void cbar_vm() { SB(); asm volatile("s_waitcnt vmcnt(0) lgkmcnt(0)\n\ts_barrier" ::: "memory"); SB(); }
__device__ __forceinline__ f32x4 mfma(u32x4 a, u32x4 b, f32x4 c) {
  return __builtin_amdgcn_mfma_f32_16x16x32_bf16(__builtin_bit_cast(bf16x8, a), __builtin_bit_cast(bf16x8, b), c, 0, 0, 0);
}
__device__ __forceinline__ unsigned cvtpk(float lo, float hi) { return __builtin_bit_cast(unsigned, __builtin_convertvector(f32x2{lo, hi}, bf16x2)); }
__device__ __forceinline__ u32x2 cvt4(f32x4 v) { return u32x2{cvtpk(v[0], v[1]), cvtpk(v[2], v[3])}; }
__device__ __forceinline__ u32x4 cvt8(f32x4 a, f32x4 b) { return u32x4{cvtpk(a[0], a[1]), cvtpk(a[2], a[3]), cvtpk(b[0], b[1]), cvtpk(b[2], b[3])}; }
__device__ __forceinline__ f32x4 bf4(unsigned lo, unsigned hi) { return f32x4{__uint_as_float(lo << 16), __uint_as_float(lo & 0xffff0000u), __uint_as_float(hi << 16), __uint_as_float(hi & 0xffff0000u)}; }
__device__ __forceinline__ u32x4 ld128(unsigned a) { return *(const LAS u32x4*)(uintptr_t)a; }
__device__ __forceinline__ f32x4 ldf4(unsigned a) { return *(const LAS f32x4*)(uintptr_t)a; }
__device__ __forceinline__ void st64(unsigned a, u32x2 v) { *(LAS u32x2*)(uintptr_t)a = v; }
__device__ __forceinline__ u32x4 ldtr(unsigned a0, unsigned a1) {
  const u32x2 lo = __builtin_bit_cast(u32x2, __builtin_amdgcn_ds_read_tr16_b64_v4i16((LAS s16x4*)(uintptr_t)a0));
  const u32x2 hi = __builtin_bit_cast(u32x2, __builtin_amdgcn_ds_read_tr16_b64_v4i16((LAS s16x4*)(uintptr_t)a1));
  return u32x4{lo[0], lo[1], hi[0], hi[1]};
}
__device__ __forceinline__ float xmax4(float x) {
  auto s = __builtin_amdgcn_permlane16_swap(__float_as_uint(x), __float_as_uint(x), false, false);
  x = fmaxf(__uint_as_float(s[0]), __uint_as_float(s[1]));
  auto t = __builtin_amdgcn_permlane32_swap(__float_as_uint(x), __float_as_uint(x), false, false);
  return fmaxf(__uint_as_float(t[0]), __uint_as_float(t[1]));
}
__device__ __forceinline__ float xsum4(float x) {
  auto s = __builtin_amdgcn_permlane16_swap(__float_as_uint(x), __float_as_uint(x), false, false);
  x = __uint_as_float(s[0]) + __uint_as_float(s[1]);
  auto t = __builtin_amdgcn_permlane32_swap(__float_as_uint(x), __float_as_uint(x), false, false);
  return __uint_as_float(t[0]) + __uint_as_float(t[1]);
}
__device__ __forceinline__ unsigned nxt(unsigned s) { s += HT; return s == RING ? 0u : s; }
__device__ __forceinline__ unsigned fresh(unsigned x) { asm volatile("" : "+v"(x)); return x; }
#define STAMP(i) do {} while (0)
#define KVMASK 0xffffffffu
#define KVAUX 2
#define EXP 2
#define THR 8.f
#define KROT 0
#define PVB 6
#define PACE2 0
#define PF 1
#define NSPIN (1 << 16)

#define TMARK(k) do {} while (0)
template <int V> struct IC { static constexpr int v = V; };

__global__ void __launch_bounds__(768) fused_k(Args a) {
  extern __shared__ __attribute__((aligned(16))) char smem[];
  const unsigned lane = threadIdx.x & 63;
  const int w = __builtin_amdgcn_readfirstlane(threadIdx.x >> 6);
  const int xcd = blockIdx.x & 7, pi = blockIdx.x >> 3;
#define PERM 0
  const int pk_ = PERM > 0 ? PERM - 1 : 0;
  const int rest = ((pi >> (pk_ + 1)) << pk_) | (pi & ((1 << pk_) - 1));
  const int idx = PERM == 0 ? pi : ((pi >> pk_) & 1) ? 8 + rest : (rest < 8 ? rest : rest + 16);
  const int wg = idx * 8 + xcd;
  const bool isA = idx < 8;
  const int nu = idx < 24 ? 3 : 2;
  const int bB = nu == 3 ? idx - 8 : (xcd & 1) * 8 + (idx - 24);
  const int slotB = nu == 3 ? xcd : 8 + (xcd >> 1);
  auto headB = [&](int u) { int x = u + bB % 3; x = x >= 3 ? x - 3 : x; return nu == 3 ? 4 * xcd + x : 4 * (xcd ^ ((u ^ bB) & 1)) + 3; };
  const int pvB = PVB;
  const unsigned L0 = (unsigned)(uintptr_t)(LAS char*)smem;

  if (w < NLD) {
    __builtin_amdgcn_s_setprio(3);
    const unsigned l = w;
    const int br = isA ? 0 : 1;
    const rsrc_t rx = mkrsrc(a.x[br], NB * SQ * DM * 2), rq = mkrsrc(a.wq[br], DM * NH * DH * 2);
    const unsigned s16 = lane & 15, e4 = lane >> 4, e2 = lane >> 5;
    auto offK = [&](int q, unsigned rs) { return (16 * l + e4) * rs + q * (4 * rs - 1024) + ((s16 ^ (4 * q + e4)) << 4); };
    auto offT = [&](int q, unsigned rs) { return (16 * l + e4) * rs + q * (4 * rs - 1024) + ((s16 ^ ((e4 ^ (((q ^ (q >> 1)) & 1) * 4)) << 1)) << 4); };
    auto offW = [&](int q, unsigned rs) { return (8 * l + e2) * rs + q * (2 * rs - 1024) + (((lane & 31) * 16) ^ (((2 * q + e2) ^ (4 * (l & 1))) * 32)); };
    unsigned vX[4], vQ[4];
#pragma unroll
    for (int q = 0; q < 4; ++q) {
      vX[q] = isA ? offK(q, 1024) : (8 * l + e4) * 1024 + q * 3072 + ((s16 ^ (8 * (l & 1) + 4 * q + e4)) << 4);
      vQ[q] = isA ? offW(q, 8192) : offT(q, 8192);
    }
    const unsigned pl = L0 + l * 4096;
    unsigned sl = 0;
    auto wt = [&](rsrc_t r, const unsigned (&v)[4], unsigned so) { dma<0, 4>(r, pl + sl, v, so); sl = nxt(sl); };
    auto kvt = [&](rsrc_t r, const unsigned (&v)[4], unsigned so) { dma<2, 4>(r, pl + sl, v, so); sl = nxt(sl); };
    unsigned vK[4], vV[4], vO[4];
    auto late = [&]() {
#pragma unroll
      for (int q = 0; q < 4; ++q) { vK[q] = offK(q, 256); vV[q] = offT(q, 256); vO[q] = offW(q, 1024); }
    };
    if (isA) {
      auto xt = [&](int i) { const unsigned c4 = ((idx >> 1) + i) & 3; dma<0, 4>(rx, pl + RING + c4 * HT, vX, (unsigned)idx * (2 * SQ * DM * 2) + c4 * 256); };
      auto qt = [&](int i) { wt(rq, vQ, ((2 * idx + (i >> 1)) & 15) * (32 * 8192) + xcd * 1024 + (i & 1) * 512); };
      TMARK(126);
      xt(0); qt(0); qt(1); qt(2); xt(1); qt(3);
      TMARK(127);
      SB(); late();
      const rsrc_t rk = mkrsrc(a.k[0], NB * 8 * KVB), rv = mkrsrc(a.v[0], NB * 8 * KVB), ro = mkrsrc(a.wo[0], DM * NH * DH * 2);
      WAITB(16); qt(4); xt(2);
      WAITB(20); qt(5); xt(3);
      WAITB(12); qt(6);
      WAITB(12); qt(7);
#pragma unroll 8
      for (int i = 8; i < 32; ++i) { WAITB(12); qt(i); }
      auto kvo = [&](int j) { return (unsigned)((2 * idx + (j >> 3)) * 8 + xcd) * KVB + ((j + KROT) & 7) * HT; };
      WAITB(12); kvt(rk, vK, kvo(0));
      WAITB(12); kvt(rk, vK, kvo(1));
      WAITB(12); kvt(rv, vV, kvo(0));
      WAITB(12); lbar(); kvt(rk, vK, kvo(2));
      WAITB(12); kvt(rv, vV, kvo(1));
#pragma unroll 1
      for (int j = 2; j < 15; ++j) {
        WAITB(12); kvt(rk, vK, kvo(j + 1));
        WAITB(12); kvt(rv, vV, kvo(j));
      }
      WAITB(12); kvt(rv, vV, kvo(15));
#pragma unroll 8
      for (int i = 0; i < 32; ++i) { WAITB(12); wt(ro, vO, xcd * (512 * 1024) + ((2 * idx + (i >> 1)) & 15) * (32 * 1024) + (i & 1) * 512); }
    } else {
      auto xt = [&](int i) { const unsigned c4 = ((bB >> 1) + i) & 3; dma<0, 2>(rx, L0 + RING + X_OFF + c4 * 8192 + l * 2048, vX, (unsigned)bB * (SQ * DM * 2) + c4 * 256); };
      auto qt = [&](int h, int i) { wt(rq, vQ, ((i + bB) & 7) * (64 * 8192) + h * 256); };
      int h = headB(0);
      TMARK(126);
      xt(0); qt(h, 0); xt(1); qt(h, 1); xt(2); qt(h, 2); xt(3); qt(h, 3);
      TMARK(127);
      SB(); late();
      const rsrc_t rk = mkrsrc(a.k[1], NB * NH * KVB), rv = mkrsrc(a.v[1], NB * NH * KVB), ro = mkrsrc(a.wo[1], DM * NH * DH * 2);
      WAITB(18); qt(h, 4);
      WAITB(16); qt(h, 5);
      WAITB(14); qt(h, 6);
      WAITB(12); qt(h, 7);
      unsigned kvo = (unsigned)(bB * NH + h) * KVB;
#pragma unroll
      for (int i = 0; i < 4; ++i) { WAITB(12); kvt(rk, vK, kvo + ((i + KROT) & 7) * HT); }
#pragma unroll 1
      for (int u = 0;; ++u) {
#pragma unroll
        for (int i = 4; i < 8; ++i) { WAITB(12); kvt(rk, vK, kvo + ((i + KROT) & 7) * HT); }
#pragma unroll
        for (int i = 0; i < PVB; ++i) { WAITB(12); if (i == 3) lbar(); kvt(rv, vV, kvo + ((i + KROT) & 7) * HT); }
        const int hn = headB(u + 1);
        if (u + 1 < nu) {
#pragma unroll
          for (int i = 0; i < 8; ++i) { WAITB(12); qt(hn, i); }
        }
#pragma unroll
        for (int i = 0; i < 8; ++i) { WAITB(12); wt(ro, vO, h * (128 * 1024) + (((i >> 1) + bB) & 3) * (32 * 1024) + (i & 1) * 512); }
#pragma unroll
        for (int i = PVB; i < 8; ++i) { WAITB(12); kvt(rv, vV, kvo + ((i + KROT) & 7) * HT); }
        if (u + 1 == nu) break;
        h = hn;
        kvo = (unsigned)(bB * NH + h) * KVB;
#pragma unroll
        for (int i = 0; i < 4; ++i) { WAITB(12); if (i == 3) lbar(); kvt(rk, vK, kvo + ((i + KROT) & 7) * HT); }
      }
    }
    WAITB(12); WAITB(8); WAITB(4); WAITB(0);
    return;
  }

  const int cw = w - NLD;
  STAMP(0);
  auto warm = [&](const void* base, unsigned off, unsigned rs) {
    const GAS char* p = (const GAS char*)base + off + (size_t)(cw * 8 + (lane >> 4)) * rs + (lane & 15) * 64;
    const unsigned t0 = *(const GAS unsigned*)p, t1 = *(const GAS unsigned*)(p + 4 * (size_t)rs);
    asm volatile("" :: "v"(t0), "v"(t1));
  };
  if (PF && nu == 2) warm(a.wq[0], (idx - 24) * (64 * 8192) + xcd * 1024, 8192);
  const u64 ep = __hip_atomic_load((const GAS u64*)a.ws, RLX);
  const u64 tag = (((u64)(unsigned)__builtin_amdgcn_readfirstlane((unsigned)(ep >> 32)) << 32) | (unsigned)__builtin_amdgcn_readfirstlane((unsigned)ep)) + 0x9E3779B97F4A7C15ull;
  const rsrc_t rt = mkrsrc(a.ws + WS_TILES, WS_TILES_BYTES);
  unsigned sl = 0;
  auto AK = [&]() { const unsigned l = fresh(lane), c = l & 15, g = l >> 4; return L0 + c * 256 + ((g ^ c) << 4); };
  auto AV = [&]() { const unsigned l = fresh(lane), g = l >> 4, qq = (l >> 2) & 3;
                    return L0 + (4 * g + qq) * 256 + (((4 * (g & 1) + qq) ^ (4 * (g >> 1))) << 5) + 8 * (l & 3); };
  auto AW = [&]() { const unsigned l = fresh(lane), g = l >> 4, qq = (l >> 2) & 3;
                    return L0 + (8 * g + qq) * 512 + ((((unsigned)cw * 2) ^ (qq ^ (4 * (g & 1)))) << 5) + 8 * (l & 3); };
  auto AF = [&]() { const unsigned l = fresh(lane); return L0 + RING + ((l & 15) + 16 * (l >> 5)) * 16 + 8 * ((l >> 4) & 1); };
  auto AL = [&]() { return L0 + RING + fresh(lane) * 16; };

  if (EXP == 3 && cw >= 4) __builtin_amdgcn_s_setprio(1);
  if (isA) {
    f32x4 acc[4][4];
    auto gemm = [&](auto xaddr, unsigned xs, bool endbar) {
#pragma unroll
      for (int i = 0; i < 4; ++i)
#pragma unroll
        for (int j = 0; j < 4; ++j) acc[i][j] = Z4;
      u32x4 xf[2][4], wf[2][2];
      const unsigned aW = AW();
      auto step = [&](int ks, int hf, int xp, bool domm, bool dord) {
        const unsigned b = aW + sl, ax = xaddr((2 * idx + ks) & 15);
        const int ph = hf ^ 1, px = hf ? xp : xp ^ 1;
#pragma unroll
        for (int i = 0; i < 4; ++i) {
          if (dord && EXP != 2) {
            if (i < 2) wf[hf][i] = ldtr(b ^ (i << 5), (b ^ (i << 5) ^ 128) + 2048);
            else if (hf == 0) { xf[xp][2 * i - 4] = ld128(ax + (2 * i - 4) * xs); xf[xp][2 * i - 3] = ld128(ax + (2 * i - 3) * xs); }
          }
          if (domm) {
            acc[ph * 2 + (i >> 1)][2 * (i & 1)] = mfma(wf[ph][i >> 1], xf[px][2 * (i & 1)], acc[ph * 2 + (i >> 1)][2 * (i & 1)]);
            acc[ph * 2 + (i >> 1)][2 * (i & 1) + 1] = mfma(wf[ph][i >> 1], xf[px][2 * (i & 1) + 1], acc[ph * 2 + (i >> 1)][2 * (i & 1) + 1]);
          }
          SB();
          if (dord && EXP == 2) {
            if (i < 2) wf[hf][i] = ldtr(b ^ (i << 5), (b ^ (i << 5) ^ 128) + 2048);
            if (i < 2 && hf == 0) { xf[xp][2 * i] = ld128(ax + (2 * i) * xs); xf[xp][2 * i + 1] = ld128(ax + (2 * i + 1) * xs); }
            SB();
          }
        }
        if (dord) sl = nxt(sl);
      };
      cbar(); if (endbar) STAMP(8); step(0, 0, 0, false, true);
      rbar(); step(0, 1, 0, true, true);
#pragma unroll 1
      for (int ks = 1; ks < 15; ks += 2) {
        rbar(); if (endbar) { if (ks == 3) STAMP(9); if (ks == 7) STAMP(10); if (ks == 11) STAMP(11); }
        step(ks, 0, 1, true, true);
        if (EXP != 1 && EXP != 4) rbar();
        step(ks, 1, 1, true, true);
        if (EXP != 4) rbar();
        step(ks + 1, 0, 0, true, true);
        if (EXP != 1 && EXP != 4) rbar();
        step(ks + 1, 1, 0, true, true);
      }
      rbar(); step(15, 0, 1, true, true);
      rbar(); step(15, 1, 1, true, true);
      if (endbar) rbar();
      step(0, 0, 0, true, false);
    };
    {
      const unsigned ax = AK() + RING;
      gemm([&](unsigned ks) { return (ax + (ks >> 2) * HT) ^ ((ks & 3) << 6); }, 4096, true);
    }
    STAMP(1);
    {
      const unsigned aF = AF() + ((cw >> 2) * 8 + (cw & 3)) * 1024;
#pragma unroll
      for (int i = 0; i < 4; ++i)
#pragma unroll
        for (int bq = 0; bq < 4; ++bq) st64(aF + (bq >> 1) * 32768 + (i >> 1) * 16384 + (bq & 1) * 4096 + (i & 1) * 512, cvt4(acc[i][bq] * SL2E));
    }
    {
      const unsigned aK = AK(), aV = AV();
      constexpr int PD = 6;
      u32x4 fr[8], qf[4], pf[2];
      f32x4 o[8], sa[4], sb[4];
      auto rdK = [&](unsigned b, int i) { fr[i & 7] = ld128((b ^ ((i & 3) << 6)) + (i >> 2) * 4096); };
      auto rdV = [&](unsigned b, int i) { const unsigned ad = (b ^ ((i & 7) << 5)) + (i >> 3) * 8192; fr[i & 7] = ldtr(ad, ad + 4096); };
      auto rdQ = [&](int bt) {
        const unsigned aq = AL() + bt * 32768 + cw * 4096;
#pragma unroll
        for (int kk = 0; kk < 4; ++kk) qf[kk] = ld128(aq + kk * 1024);
      };
      auto one = [](float x) { asm volatile("" : "+v"(x)); return x; };
      f32x4 nm = Z4, ls = Z4;
      float mx;
      bool fresh_rows = true;
      const u32x4 ones = {0x3f803f80u, 0x3f803f80u, 0x3f803f80u, 0x3f803f80u};
      auto phaseA = [&](f32x4 (&sn)[4], f32x4 (&sc)[4], bool domm, bool dosm, bool nextK) {
        const unsigned bc = sl + aK;
        if (domm) sl = nxt(sl);
        const unsigned bn = sl + (nextK ? aK : aV);
#pragma unroll
        for (int i = 0; i < 16; ++i) {
          if (domm) {
            if (i == 16 - PD) rbar();
            sn[i >> 2] = mfma(fr[i & 7], qf[i & 3], (i & 3) ? sn[i >> 2] : nm);
            if (i + PD < 16) rdK(bc, i + PD); else if (nextK) rdK(bn, i + PD - 16); else rdV(bn, i + PD - 16);
          }
          if (dosm) {
            sc[i >> 2][i & 3] = __builtin_amdgcn_exp2f(sc[i >> 2][i & 3]);
            if (i & 1) pf[i >> 3][(i >> 1) & 3] = cvtpk(sc[i >> 2][(i & 3) - 1], sc[i >> 2][i & 3]);
          }
          if (i & 1) SB();
        }
      };
      auto phaseB = [&](f32x4 (&sn)[4], bool bar, bool nextK) {
        const unsigned bc = sl + aV;
        sl = nxt(sl);
        const unsigned bn = sl + (nextK ? aK : aV);
#pragma unroll
        for (int i = 0; i < 16; ++i) {
          if (i == 16 - PD && bar) rbar();
          o[i & 7] = mfma(fr[i & 7], pf[i >> 3], o[i & 7]);
          if (i + PD < 16) rdV(bc, i + PD); else if (nextK) rdK(bn, i + PD - 16); else rdV(bn, i + PD - 16);
          if ((i & 7) == 3) ls = mfma(ones, pf[i >> 3], ls);
          if (i & 1) { asm("v_max3_f32 %0, %1, %2, %3" : "=v"(mx) : "v"(sn[i >> 2][(i & 3) - 1]), "v"(sn[i >> 2][i & 3]), "v"(i == 1 ? sn[0][0] : mx)); SB(); }
        }
      };
      auto check = [&](f32x4 (&sn)[4]) {
        if (fresh_rows || __any(!(mx <= THR))) {
          float d = xmax4(mx);
          if (!fresh_rows) d = fmaxf(d, 0.f);
          const float alpha = fresh_rows ? 1.f : __builtin_amdgcn_exp2f(-d);
          nm -= d; ls *= alpha;
#pragma unroll
          for (int i = 0; i < 8; ++i) o[i] *= alpha;
#pragma unroll
          for (int i = 0; i < 4; ++i) sn[i] -= d;
          fresh_rows = false;
        }
        SB();
      };
      auto finish = [&](int bt) {
        const float inv = __builtin_amdgcn_rcpf(ls[0]);
        const unsigned aF = AF() + ((bt * 2 + (cw & 1)) * 16 + (cw >> 1) * 4) * 1024;
#pragma unroll
        for (int db = 0; db < 8; ++db) { st64(aF + db * 512, cvt4(o[db] * inv)); o[db] = Z4; }
        ls = Z4; fresh_rows = true;
      };
#pragma unroll
      for (int i = 0; i < 8; ++i) o[i] = Z4;
      cbar();
      rdQ(0);
#pragma unroll
      for (int i = 0; i < PD; ++i) rdK(aK + sl, i);
      phaseA(sa, sb, true, false, true);
      mx = -INFINITY;
#pragma unroll
      for (int i = 0; i < 4; ++i) mx = fmaxf(fmaxf(fmaxf(sa[i][0], sa[i][1]), mx), fmaxf(sa[i][2], sa[i][3]));
#pragma unroll 1
      for (int j = 0; j < 14; j += 2) {
        check(sa); phaseA(sb, sa, true, true, false);
        if (j == 6) rdQ(1);
        phaseB(sb, true, true);
        check(sb); phaseA(sa, sb, true, true, false); phaseB(sa, true, true);
        if (j == 6) { finish(0); STAMP(2); }
      }
      check(sa); phaseA(sb, sa, true, true, false); phaseB(sb, true, false);
      check(sb); phaseA(sa, sb, false, true, false); phaseB(sa, false, false);
      finish(1);
      STAMP(3);
    }
    {
      const unsigned al = AL();
      gemm([&](unsigned ks) { return al + ks * 1024; }, 16384, false);
    }
    STAMP(4);
#pragma unroll
    for (int bq = 0; bq < 4; ++bq)
#pragma unroll
      for (int hf = 0; hf < 2; ++hf)
        __builtin_amdgcn_raw_buffer_store_b128(cvt8(acc[2 * hf][bq], acc[2 * hf + 1][bq]), rt, lane * 16,
                                               WS_TILES_A + ((2 * idx + (bq >> 1)) * 8 + xcd) * TILE_B + ((hf * 8 + cw) * 2 + (bq & 1)) * 1024, 16);
    cbar_vm();
    if (cw == 0 && lane < 2) __hip_atomic_store((GAS u64*)(a.ws + WS_FLAGA + ((2 * idx + lane) * 8 + xcd) * 8), tag, RLX);
  } else {
    f32x4 oa[4][2];
#pragma unroll
    for (int nb = 0; nb < 4; ++nb) { oa[nb][0] = Z4; oa[nb][1] = Z4; }
    unsigned aq, ax0;
    {
      const unsigned l = fresh(lane), g = l >> 4, qq = (l >> 2) & 3;
      aq = L0 + (8 * g + qq) * 256 + (((unsigned)cw ^ (qq ^ (4 * (g & 1)))) << 5) + 8 * (l & 3);
      ax0 = AK() + RING + X_OFF;
    }
    const unsigned al = AL(), aF = AF() + cw * 512;
    f32x4 qa[2];
    u32x4 wf[2][2], xf[2][4];
    auto qrd = [&](int st, int p) {
      const unsigned ii = (st + bB) & 7, b = aq + sl, ax = (ax0 + (ii >> 1) * 8192) ^ ((ii & 1) << 7);
#pragma unroll
      for (int ks = 0; ks < 2; ++ks) wf[p][ks] = ldtr(b + ks * 8192, (b ^ 128) + 1024 + ks * 8192);
#pragma unroll
      for (int i = 0; i < 4; ++i) xf[p][i] = ld128((ax ^ ((i >> 1) << 6)) + (i & 1) * 4096);
      sl = nxt(sl);
      SB();
    };
    auto qmm = [&](int p) {
#pragma unroll
      for (int i = 0; i < 4; ++i) qa[i & 1] = mfma(wf[p][i >> 1], xf[p][i], qa[i & 1]);
      SB();
    };
    auto qrest = [&]() {
#pragma unroll 1
      for (int st = 1; st < 7; st += 2) {
        rbar(); qrd(st, 1); qmm(0);
        rbar(); qrd(st + 1, 0); qmm(1);
      }
      rbar(); qrd(7, 1); qmm(0);
    };
    auto qwr = [&]() {
#pragma unroll
      for (int qb = 0; qb < 2; ++qb) st64(aF + Q_OFF + qb * 4096, cvt4(qa[qb]));
      SB();
    };
    qa[0] = Z4; qa[1] = Z4;
    rbar(); qrd(0, 0); qrest(); qmm(1); qwr();
    STAMP(8);
#pragma unroll 1
    for (int u = 0; u < nu; ++u) {
      f32x4 s[8];
      {
        u32x4 qf[4], kf[2][4];
        const unsigned aK = ax0 - (RING + X_OFF) + (cw >> 1) * 4096;
#pragma unroll
        for (int i = 0; i <= 8; ++i) {
          if (i < 8) {
            if (i == 0) cbar(); else rbar();
            const unsigned b = aK + sl;
#pragma unroll
            for (int kk = 0; kk < 4; ++kk) kf[i & 1][kk] = ld128(b ^ (kk << 6));
            if (i == 0) {
#pragma unroll
              for (int kk = 0; kk < 4; ++kk) qf[kk] = ld128(al + (Q_OFF + (cw & 1) * 4096) + kk * 1024);
            }
            sl = nxt(sl);
            SB();
          }
          if (i > 0) {
            s[i - 1] = Z4;
#pragma unroll
            for (int kk = 0; kk < 4; ++kk) s[i - 1] = mfma(kf[(i - 1) & 1][kk], qf[kk], s[i - 1]);
            SB();
          }
        }
      }
      if (u == 1) STAMP(1);
      const unsigned aS = L0 + RING + ST_OFF + (fresh(lane) & 15) * 16;
      {
        float mx = s[0][0];
#pragma unroll
        for (int i = 0; i < 8; ++i)
#pragma unroll
          for (int r = 0; r < 4; ++r) mx = fmaxf(mx, s[i][r]);
        mx = xmax4(mx);
        if (lane < 16) *(LAS float*)(uintptr_t)(aS + (cw & 1) * 256 + (cw >> 1) * 4) = mx;
      }
      cbar();
      u32x2 pk[4];
      const unsigned aP = al + (P_OFF + (cw >> 2) * 2048 + (cw & 1) * 1024 + ((cw >> 1) & 1) * 8);
      {
        const f32x4 m0 = ldf4(aS + (cw & 1) * 256);
        const float mn = SL2E * fmaxf(fmaxf(m0[0], m0[1]), fmaxf(m0[2], m0[3]));
        f32x4 ps = Z4;
#pragma unroll
        for (int i = 0; i < 8; ++i) {
          const f32x4 e = s[i] * SL2E - mn;
#pragma unroll
          for (int r = 0; r < 4; ++r) s[i][r] = __builtin_amdgcn_exp2f(e[r]);
          ps += s[i];
          if (i < 4) st64(aP + i * 4096, cvt4(s[i])); else pk[i - 4] = cvt4(s[i]);
        }
        const float pt = xsum4((ps[0] + ps[1]) + (ps[2] + ps[3]));
        if (lane < 16) *(LAS float*)(uintptr_t)(aS + 512 + (cw & 1) * 256 + (cw >> 1) * 4) = pt;
      }
      f32x4 o[2] = {Z4, Z4};
      float inv[2];
      const unsigned aV = AV() ^ ((unsigned)cw << 5), aw = AW();
      u32x4 vf[2][2], pf[2][4], wo[4][4];
      auto vrd = [&](int i) {
        if (i >= 2 && i < 6) st64(aP + (i - 2) * 4096, pk[i - 2]);
        SB();
        const unsigned b = aV + sl;
#pragma unroll
        for (int ks = 0; ks < 2; ++ks) vf[i & 1][ks] = ldtr(b + ks * 8192, b + ks * 8192 + 4096);
#pragma unroll
        for (int j = 0; j < 4; ++j) pf[i & 1][j] = ld128(al + (P_OFF + (i & 3) * 4096 + j * 1024));
        sl = nxt(sl);
        SB();
      };
      auto vmm = [&](int i) {
#pragma unroll
        for (int j = 0; j < 4; ++j) o[j & 1] = mfma(vf[i & 1][j >> 1], pf[i & 1][j], o[j & 1]);
        SB();
      };
      auto wrd = [&](int i) {
        const unsigned b = aw + sl;
#pragma unroll
        for (int nb = 0; nb < 2; ++nb) wo[i >> 1][(i & 1) * 2 + nb] = ldtr(b ^ (nb << 5), (b ^ (nb << 5) ^ 128) + 2048);
        sl = nxt(sl);
        SB();
      };
#pragma unroll
      for (int i = 0; i < PVB; ++i) {
        if (i == 0) cbar(); else rbar();
        vrd(i);
        if (i > 0) vmm(i - 1);
        if (i == 3) {
#pragma unroll
          for (int qb = 0; qb < 2; ++qb) { const f32x4 l0 = ldf4(aS + 512 + qb * 256); inv[qb] = __builtin_amdgcn_rcpf((l0[0] + l0[1]) + (l0[2] + l0[3])); }
          SB();
        }
      }
      if (u + 1 < nu) {
        qa[0] = Z4; qa[1] = Z4;
        rbar(); if (u == 1) STAMP(9); qrd(0, 0); vmm(PVB - 1);
        qrest();
        rbar(); if (u == 1) STAMP(10); wrd(0); qmm(1);
        rbar(); wrd(1); qwr();
      } else {
        rbar(); wrd(0); vmm(PVB - 1);
        rbar(); wrd(1);
      }
#pragma unroll
      for (int i = 2; i < 8; ++i) {
        SB(); asm volatile("s_waitcnt lgkmcnt(4)\n\ts_barrier" ::: "memory"); SB();
        wrd(i);
      }
#pragma unroll
      for (int i = PVB; i < 8; ++i) {
        if (i == PVB && u == 1) STAMP(11);
        if (i == PVB) cbar(); else rbar();
        vrd(i); vmm(i);
      }
#pragma unroll
      for (int qb = 0; qb < 2; ++qb) st64(aF + P_OFF + qb * 4096, cvt4(o[qb] * inv[qb]));
      cbar();
      {
        u32x4 of[2][4];
#pragma unroll
        for (int kk = 0; kk < 4; ++kk)
#pragma unroll
          for (int qb = 0; qb < 2; ++qb) of[qb][kk] = ld128(al + ((kk + bB) & 3) * 1024 + (P_OFF + qb * 4096));
        SB();
#pragma unroll
        for (int kk = 0; kk < 4; ++kk)
#pragma unroll
          for (int qb = 0; qb < 2; ++qb)
#pragma unroll
            for (int nb = 0; nb < 4; ++nb) oa[nb][qb] = mfma(wo[kk][nb], of[qb][kk], oa[nb][qb]);
      }
      STAMP(2 + u);
    }
#pragma unroll
    for (int qb = 0; qb < 2; ++qb)
#pragma unroll
      for (int hf = 0; hf < 2; ++hf)
        __builtin_amdgcn_raw_buffer_store_b128(cvt8(oa[2 * hf][qb], oa[2 * hf + 1][qb]), rt, lane * 16, (bB * 12 + slotB) * TILE_B + ((hf * 8 + cw) * 2 + qb) * 1024, 16);
    cbar_vm();
    if (cw == 0 && lane == 0) __hip_atomic_store((GAS u64*)(a.ws + WS_FLAGB + (bB * 16 + slotB) * 8), tag, RLX);
  }
  STAMP(5);

  {
    const unsigned c = lane & 15, g = lane >> 4;
    const int rb = wg >> 4, pc = (wg & 15) * 2 + ((cw >> 1) & 1), tg = cw & 1;
    const bool wB = cw < 4;
    const unsigned nt = wB ? 6 : 4;
    const unsigned fl = lane < nt ? lane : 0;
    const GAS u64* fp = (const GAS u64*)(a.ws + (wB ? WS_FLAGB + (rb * 16 + tg * 6 + fl) * 8 : WS_FLAGA + (rb * 8 + tg * 4 + fl) * 8));
    for (int spin = 0; spin < NSPIN; ++spin) {
      if (__all(__hip_atomic_load(fp, RLX) == tag)) break;
      __builtin_amdgcn_s_sleep(4);
    }
    __builtin_amdgcn_fence(__ATOMIC_ACQUIRE, "wavefront");
    STAMP(6);
    const unsigned so = (wB ? (rb * 12 + tg * 6) * TILE_B : WS_TILES_A + (rb * 8 + tg * 4) * TILE_B) + pc * 1024;
    u32x4 t[6];
#pragma unroll
    for (int i = 0; i < 6; ++i) t[i] = __builtin_amdgcn_raw_buffer_load_b128(rt, (i < 4 || wB) ? lane * 16 : 0x7fffff00u, so + i * TILE_B, 16);
    f32x4 s0 = Z4, s1 = Z4;
#pragma unroll
    for (int i = 0; i < 6; ++i) { s0 += bf4(t[i][0], t[i][1]); s1 += bf4(t[i][2], t[i][3]); }
    *(LAS f32x4*)(uintptr_t)(L0 + cw * 1024 + lane * 16) = tg ? s0 : s1;
    cbar();
    const f32x4 oth = ldf4(L0 + (cw ^ 1) * 1024 + lane * 16);
    const f32x4 r = tg ? oth + s1 : s0 + oth;
    *(GAS u32x2*)(a.out + ((size_t)(rb * SQ + (pc & 1) * 16 + c) * 1024 + (wB ? 512 : 0) + 32 * (pc >> 1) + 16 * tg + 4 * g) * 2) = cvt4(r);
    if (wg == 255 && cw == 1) {
      for (int spin = 0; spin < NSPIN; ++spin) {
        bool ok = true;
#pragma unroll
        for (int i = 0; i < 4; ++i) ok &= (lane & 15) >= 12 || __hip_atomic_load((const GAS u64*)(a.ws + WS_FLAGB + (i * 64 + lane) * 8), RLX) == tag;
#pragma unroll
        for (int i = 0; i < 2; ++i) ok &= __hip_atomic_load((const GAS u64*)(a.ws + WS_FLAGA + (i * 64 + lane) * 8), RLX) == tag;
        if (__all(ok)) break;
        __builtin_amdgcn_s_sleep(2);
      }
      if (lane == 0) __hip_atomic_store((GAS u64*)a.ws, tag, RLX);
    }
  }
  STAMP(7);
}

extern "C" void kernel_launch(void* const* d_in, const int* in_sizes, int n_in, void* d_out, int out_size, void* d_ws, size_t ws_size,
                              hipStream_t stream) {
  Args a;
  for (int br = 0; br < 2; ++br) {
    a.x[br] = d_in[5 * br + 0];
    a.k[br] = d_in[5 * br + 1];
    a.v[br] = d_in[5 * br + 2];
    a.wq[br] = d_in[5 * br + 3];
    a.wo[br] = d_in[5 * br + 4];
  }
  a.ws = (char*)d_ws;
  a.out = (char*)d_out;
  (void)hipFuncSetAttribute((const void*)fused_k, hipFuncAttributeMaxDynamicSharedMemorySize, LDS_BYTES);
  fused_k<<<256, 768, LDS_BYTES, stream>>>(a);
}
```

```cpp
#include <hip/hip_runtime.h>
#include <stdint.h>

#define LAS __attribute__((address_space(3)))
#define GAS __attribute__((address_space(1)))
typedef unsigned u32x4 __attribute__((ext_vector_type(4)));
typedef unsigned u32x2 __attribute__((ext_vector_type(2)));
typedef float f32x4 __attribute__((ext_vector_type(4)));
typedef float f32x2 __attribute__((ext_vector_type(2)));
typedef short bf16x8 __attribute__((ext_vector_type(8)));
typedef short s16x4 __attribute__((ext_vector_type(4)));
typedef __bf16 bf16x2 __attribute__((ext_vector_type(2)));
typedef __amdgpu_buffer_rsrc_t rsrc_t;
typedef unsigned long long u64;

constexpr int NB = 16, SQ = 32, DM = 512, NH = 32, SK = 512, DH = 128;
constexpr float SL2E = 0.088388347648318447f * 1.4426950408889634f;
constexpr unsigned HT = 16384, RING = 6 * HT;
constexpr unsigned Q_OFF = 16384, ST_OFF = Q_OFF + 8192;
constexpr unsigned LDS_BYTES = 160 * 1024;
constexpr int NLD = 4;
constexpr unsigned KVB = SK * DH * 2;
constexpr unsigned WS_FLAG = 4096, WS_TILES = 1u << 20, TILE_B = 32768;
constexpr unsigned WS_TILES_A = 192 * TILE_B, WS_TILES_BYTES = 320 * TILE_B;

struct Args {
  const void* x[2]; const void* k[2]; const void* v[2]; const void* wq[2]; const void* wo[2];
  char* ws; char* out;
};

#define SB() __builtin_amdgcn_sched_barrier(0)
#define INL __attribute__((always_inline))
#define TAGC 0x9E3779B97F4A7C15ull
#define RLX __ATOMIC_RELAXED, __HIP_MEMORY_SCOPE_AGENT
#define Z4 f32x4{0.f, 0.f, 0.f, 0.f}
__device__ __forceinline__ rsrc_t mkrsrc(const void* p, unsigned bytes) { return __builtin_amdgcn_make_buffer_rsrc((void*)p, 0, bytes, 0x00020000); }
template <int AUX, int N> __device__ __forceinline__ void dma(rsrc_t r, unsigned lds, const unsigned (&v)[4], unsigned soff) {
  __builtin_amdgcn_raw_ptr_buffer_load_lds(r, (LAS void*)(uintptr_t)lds, 16, v[0], soff, 0, AUX);
  __builtin_amdgcn_raw_ptr_buffer_load_lds(r, (LAS void*)(uintptr_t)lds, 16, v[1], soff, 1024, AUX);
  if (N > 2) {
    __builtin_amdgcn_raw_ptr_buffer_load_lds(r, (LAS void*)(uintptr_t)lds, 16, v[2], soff, 2048, AUX);
    __builtin_amdgcn_raw_ptr_buffer_load_lds(r, (LAS void*)(uintptr_t)lds, 16, v[3], soff, 3072, AUX);
  }
}
template <int N> __device__ __forceinline__ void waitb() { asm volatile("s_waitcnt vmcnt(%0)\n\ts_barrier" :: "i"(N) : "memory"); }
#define WAITB(N) waitb<N>()
__device__ __forceinline__ void lbar() { asm volatile("s_barrier" ::: "memory"); }
__device__ __forceinline__ void rbar() { SB(); asm volatile("s_barrier" ::: "memory"); SB(); }
__device__ __forceinline__ void cbar() { SB(); asm volatile("s_waitcnt lgkmcnt(0)\n\ts_barrier" ::: "memory"); SB(); }
__device__ __forceinline__ void cbar_vm() { SB(); asm volatile("s_waitcnt vmcnt(0) lgkmcnt(0)\n\ts_barrier" ::: "memory"); SB(); }
__device__ __forceinline__ f32x4 mfma(u32x4 a, u32x4 b, f32x4 c) {
  return __builtin_amdgcn_mfma_f32_16x16x32_bf16(__builtin_bit_cast(bf16x8, a), __builtin_bit_cast(bf16x8, b), c, 0, 0, 0);
}
typedef float f32x16 __attribute__((ext_vector_type(16)));
__device__ __forceinline__ f32x16 mfma32(u32x4 a, u32x4 b, f32x16 c) {
  return __builtin_amdgcn_mfma_f32_32x32x16_bf16(__builtin_bit_cast(bf16x8, a), __builtin_bit_cast(bf16x8, b), c, 0, 0, 0);
}
__device__ __forceinline__ unsigned cvtpk(float lo, float hi) { return __builtin_bit_cast(unsigned, __builtin_convertvector(f32x2{lo, hi}, bf16x2)); }
__device__ __forceinline__ u32x2 cvt4(f32x4 v) { return u32x2{cvtpk(v[0], v[1]), cvtpk(v[2], v[3])}; }
__device__ __forceinline__ u32x4 cvt8(f32x4 a, f32x4 b) { return u32x4{cvtpk(a[0], a[1]), cvtpk(a[2], a[3]), cvtpk(b[0], b[1]), cvtpk(b[2], b[3])}; }
__device__ __forceinline__ f32x4 bf4(unsigned lo, unsigned hi) { return f32x4{__uint_as_float(lo << 16), __uint_as_float(lo & 0xffff0000u), __uint_as_float(hi << 16), __uint_as_float(hi & 0xffff0000u)}; }
__device__ __forceinline__ u32x4 ld128(unsigned a) { return *(const LAS u32x4*)(uintptr_t)a; }
__device__ __forceinline__ f32x4 ldf4(unsigned a) { return *(const LAS f32x4*)(uintptr_t)a; }
__device__ __forceinline__ void st64(unsigned a, u32x2 v) { *(LAS u32x2*)(uintptr_t)a = v; }
__device__ __forceinline__ u32x4 ldtr(unsigned a0, unsigned a1) {
  const u32x2 lo = __builtin_bit_cast(u32x2, __builtin_amdgcn_ds_read_tr16_b64_v4i16((LAS s16x4*)(uintptr_t)a0));
  const u32x2 hi = __builtin_bit_cast(u32x2, __builtin_amdgcn_ds_read_tr16_b64_v4i16((LAS s16x4*)(uintptr_t)a1));
  return u32x4{lo[0], lo[1], hi[0], hi[1]};
}
__device__ __forceinline__ float xmax4(float x) {
  auto s = __builtin_amdgcn_permlane16_swap(__float_as_uint(x), __float_as_uint(x), false, false);
  x = fmaxf(__uint_as_float(s[0]), __uint_as_float(s[1]));
  auto t = __builtin_amdgcn_permlane32_swap(__float_as_uint(x), __float_as_uint(x), false, false);
  return fmaxf(__uint_as_float(t[0]), __uint_as_float(t[1]));
}
__device__ __forceinline__ float xsum4(float x) {
  auto s = __builtin_amdgcn_permlane16_swap(__float_as_uint(x), __float_as_uint(x), false, false);
  x = __uint_as_float(s[0]) + __uint_as_float(s[1]);
  auto t = __builtin_amdgcn_permlane32_swap(__float_as_uint(x), __float_as_uint(x), false, false);
  return __uint_as_float(t[0]) + __uint_as_float(t[1]);
}
__device__ __forceinline__ unsigned nxt(unsigned s) { s += HT; return s == RING ? 0u : s; }
__device__ __forceinline__ unsigned fresh(unsigned x) { asm volatile("" : "+v"(x)); return x; }
#define STAMP(i) do {} while (0)
#define KVMASK 0xffffffffu
#define KVAUX 2
#define EXP 2
#define THR 8.f
#define KROT 0
#define SLP 2
#define PACES 0
#define YSEL (nu == 3)
#define PVB 6
#define PACE2 0
#define PF 1
#define NSPIN (1 << 16)

#define TMARK(k) do {} while (0)
#define WGI (idx * 8 + xcd)

__global__ void __launch_bounds__(768) fused_k(Args a) {
  extern __shared__ __attribute__((aligned(16))) char smem[];
  const unsigned lane = threadIdx.x & 63;
  const int w = __builtin_amdgcn_readfirstlane(threadIdx.x >> 6);
  const int xcd = blockIdx.x & 7, pi = blockIdx.x >> 3;
  const int idx = (pi & 1) ? 8 + (pi >> 1) : (pi < 16 ? pi >> 1 : 16 + (pi >> 1));
  const bool isA = idx < 8;
  const int nu = idx < 24 ? 3 : 2;
  const int bB = nu == 3 ? idx - 8 : (xcd & 1) * 8 + (idx - 24);
  const int slotB = nu == 3 ? xcd : 8 + (xcd >> 1);
  auto headB = [&](int u) INL { int x = u + bB % 3; x = x >= 3 ? x - 3 : x; return nu == 3 ? 4 * xcd + x : 4 * (xcd ^ ((u ^ bB) & 1)) + 3; };
  const bool early = YSEL;
  constexpr unsigned poff = 32768;
  const unsigned L0 = (unsigned)(uintptr_t)(LAS char*)smem;

  if (__builtin_expect(w < NLD, 1)) {
    const unsigned l = w;
    const rsrc_t rx = mkrsrc(isA ? a.x[0] : a.x[1], NB * SQ * DM * 2), rq = mkrsrc(isA ? a.wq[0] : a.wq[1], DM * NH * DH * 2);
    const unsigned s16 = lane & 15, e4 = lane >> 4, e2 = lane >> 5;
    auto offK = [&](int q, unsigned rs) INL { return (16 * l + e4) * rs + q * (4 * rs - 1024) + ((s16 ^ (4 * q + e4)) << 4); };
    auto offT = [&](int q, unsigned rs) INL { return (16 * l + e4) * rs + q * (4 * rs - 1024) + ((s16 ^ ((e4 ^ (((q ^ (q >> 1)) & 1) * 4)) << 1)) << 4); };
    auto offW = [&](int q, unsigned rs) INL { return (8 * l + e2) * rs + q * (2 * rs - 1024) + (((lane & 31) * 16) ^ (((2 * q + e2) ^ (4 * (l & 1))) * 32)); };
    const unsigned pl = L0 + l * 4096;
    unsigned sl = 0;
    auto wt = [&](rsrc_t r, const unsigned (&v)[4], unsigned so) INL { dma<0, 4>(r, pl + sl, v, so); sl = nxt(sl); };
    auto kvt = [&](rsrc_t r, const unsigned (&v)[4], unsigned so) INL {
      if (PACES && nu == 2) __builtin_amdgcn_s_sleep(PACES);
      dma<2, 4>(r, pl + sl, v, so); sl = nxt(sl);
    };
    unsigned vX[4], vQ[4], vK[4], vV[4], vO[4];
    auto late = [&]() INL {
      __builtin_amdgcn_s_setprio(3);
      SB();
#pragma unroll
      for (int q = 0; q < 4; ++q) { vK[q] = fresh(offK(q, 256)); vV[q] = fresh(offT(q, 256)); vO[q] = fresh(offW(q, 1024)); }
      asm volatile("" :: "s"(a.k[0]), "s"(a.k[1]), "s"(a.v[0]), "s"(a.v[1]), "s"(a.wo[0]), "s"(a.wo[1]));
      SB();
    };
    if (__builtin_expect(!isA, 1)) {
#pragma unroll
      for (int q = 0; q < 4; ++q) { vX[q] = (8 * l + e4) * 1024 + q * 3072 + ((s16 ^ (8 * (l & 1) + 4 * q + e4)) << 4); vQ[q] = offT(q, 8192); }
      auto xt = [&](int i) INL { const unsigned c4 = ((bB >> 1) + i) & 3; dma<0, 2>(rx, L0 + RING + c4 * 8192 + l * 2048, vX, (unsigned)bB * (SQ * DM * 2) + c4 * 256); };
      auto qt = [&](int h, int i) INL { wt(rq, vQ, ((i + bB) & 7) * (64 * 8192) + h * 256); };
      int h = headB(0);
      TMARK(126);
      xt(0); qt(h, 0); xt(1); qt(h, 1); xt(2); qt(h, 2); xt(3); qt(h, 3);
      TMARK(127);
      late();
      WAITB(18); qt(h, 4);
      WAITB(16); qt(h, 5);
      WAITB(14); qt(h, 6);
      WAITB(12); qt(h, 7);
      {
        const rsrc_t rk = mkrsrc(a.k[1], NB * NH * KVB), rv = mkrsrc(a.v[1], NB * NH * KVB), ro = mkrsrc(a.wo[1], DM * NH * DH * 2);
        unsigned kvo = (unsigned)(bB * NH + h) * KVB;
        int hp = h;
        auto ot = [&](int hh, int i) INL { wt(ro, vO, hh * (128 * 1024) + (((i >> 1) + bB) & 3) * (32 * 1024) + (i & 1) * 512); };
#pragma unroll
        for (int i = 0; i < 6; ++i) { WAITB(12); kvt(rk, vK, kvo + i * HT); }
#pragma unroll 1
        for (int u = 0;; ++u) {
          const int hn = headB(u + 1);
          if (early && u > 0) {
#pragma unroll
            for (int i = 0; i < 8; ++i) { WAITB(12); ot(hp, i); }
          }
          if (early && u + 1 < nu) {
#pragma unroll
            for (int i = 0; i < 8; ++i) { WAITB(12); qt(hn, i); }
          }
          WAITB(12); kvt(rk, vK, kvo + 6 * HT);
          WAITB(12); kvt(rk, vK, kvo + 7 * HT);
#pragma unroll
          for (int i = 0; i < 3; ++i) { WAITB(12); kvt(rv, vV, kvo + i * HT); }
          WAITB(12); lbar();
#pragma unroll
          for (int i = 3; i < 6; ++i) kvt(rv, vV, kvo + i * HT);
          WAITB(20); WAITB(16);
          if (!early && u + 1 < nu) {
#pragma unroll
            for (int i = 0; i < 8; ++i) { WAITB(12); qt(hn, i); }
          }
          if (!early || u + 1 == nu) {
#pragma unroll
            for (int i = 0; i < 8; ++i) { WAITB(12); ot(h, i); }
          }
          WAITB(12); kvt(rv, vV, kvo + 6 * HT);
          WAITB(12); kvt(rv, vV, kvo + 7 * HT);
          if (u + 1 == nu) break;
          hp = h; h = hn;
          kvo = (unsigned)(bB * NH + h) * KVB;
#pragma unroll
          for (int i = 0; i < 3; ++i) { WAITB(12); kvt(rk, vK, kvo + i * HT); }
          WAITB(12); lbar();
#pragma unroll
          for (int i = 3; i < 6; ++i) kvt(rk, vK, kvo + i * HT);
          WAITB(20); WAITB(16);
        }
      }
    } else {
#pragma unroll
      for (int q = 0; q < 4; ++q) { vX[q] = offK(q, 1024); vQ[q] = offW(q, 8192); }
      auto xt = [&](int i) INL { const unsigned c4 = ((idx >> 1) + i) & 3; dma<0, 4>(rx, pl + RING + c4 * HT, vX, (unsigned)idx * (2 * SQ * DM * 2) + c4 * 256); };
      auto qt = [&](int i) INL { wt(rq, vQ, ((2 * idx + (i >> 1)) & 15) * (32 * 8192) + xcd * 1024 + (i & 1) * 512); };
      TMARK(126);
      xt(0); qt(0); qt(1); qt(2); xt(1); qt(3);
      TMARK(127);
      late();
      const rsrc_t rk = mkrsrc(a.k[0], NB * 8 * KVB), rv = mkrsrc(a.v[0], NB * 8 * KVB), ro = mkrsrc(a.wo[0], DM * NH * DH * 2);
      WAITB(16); qt(4); xt(2);
      WAITB(20); qt(5); xt(3);
      WAITB(12); qt(6);
      WAITB(12); qt(7);
#pragma unroll 4
      for (int i = 8; i < 32; ++i) { WAITB(12); qt(i); }
      auto kvo = [&](int j) INL { return (unsigned)((2 * idx + (j >> 3)) * 8 + xcd) * KVB + (j & 7) * HT; };
      WAITB(12); kvt(rk, vK, kvo(0));
      WAITB(12); kvt(rk, vK, kvo(1));
      WAITB(12); kvt(rv, vV, kvo(0));
      WAITB(12); lbar(); kvt(rk, vK, kvo(2));
      WAITB(12); kvt(rv, vV, kvo(1));
#pragma unroll 1
      for (int j = 2; j < 15; ++j) {
        WAITB(12); kvt(rk, vK, kvo(j + 1));
        WAITB(12); kvt(rv, vV, kvo(j));
      }
      WAITB(12); kvt(rv, vV, kvo(15));
#pragma unroll 4
      for (int i = 0; i < 32; ++i) { WAITB(12); wt(ro, vO, xcd * (512 * 1024) + ((2 * idx + (i >> 1)) & 15) * (32 * 1024) + (i & 1) * 512); }
    }
    WAITB(12); WAITB(8); WAITB(4); WAITB(0);
    if (blockIdx.x == 240 && w == 0) {
      lbar();
      const u64 tag = __hip_atomic_load((const GAS u64*)a.ws, RLX) + TAGC;
      const unsigned sidx = lane & 31, su = (sidx < 12 || (sidx >= 16 && sidx < 24)) ? sidx : 0;
      for (int spin = 0; spin < NSPIN; ++spin) {
        bool ok = true;
#pragma unroll
        for (int i = 0; i < 8; ++i) ok &= __hip_atomic_load((const GAS u64*)(a.ws + WS_FLAG + ((i * 2 + (lane >> 5)) * 256 + su) * 8), RLX) == tag;
        if (__all(ok)) break;
        __builtin_amdgcn_s_sleep(4);
      }
      if (lane == 0) __hip_atomic_store((GAS u64*)a.ws, tag, RLX);
    }
    return;
  }

  const int cw = w - NLD;
  STAMP(0);
  if (PF && nu == 2) {
    const GAS char* p = (const GAS char*)a.wq[0] + (idx - 24) * (64 * 8192) + xcd * 1024 + (size_t)(cw * 8 + (lane >> 4)) * 8192 + (lane & 15) * 64;
    const unsigned t0 = *(const GAS unsigned*)p, t1 = *(const GAS unsigned*)(p + 4 * 8192);
    asm volatile("" :: "v"(t0), "v"(t1));
  }
  const u64 ep = __hip_atomic_load((const GAS u64*)a.ws, RLX);
  const u64 tag = (((u64)(unsigned)__builtin_amdgcn_readfirstlane((unsigned)(ep >> 32)) << 32) | (unsigned)__builtin_amdgcn_readfirstlane((unsigned)ep)) + TAGC;
  unsigned sl = 0;
  auto AK = [&]() INL { const unsigned l = fresh(lane), c = l & 15, g = l >> 4; return L0 + c * 256 + ((g ^ c) << 4); };
  auto AV = [&]() INL { const unsigned l = fresh(lane), g = l >> 4, qq = (l >> 2) & 3;
                    return L0 + (4 * g + qq) * 256 + (((4 * (g & 1) + qq) ^ (4 * (g >> 1))) << 5) + 8 * (l & 3); };
  auto AW = [&]() INL { const unsigned l = fresh(lane), g = l >> 4, qq = (l >> 2) & 3;
                    return L0 + (8 * g + qq) * 512 + ((((unsigned)cw * 2) ^ (qq ^ (4 * (g & 1)))) << 5) + 8 * (l & 3); };
  auto AF = [&]() INL { const unsigned l = fresh(lane); return L0 + RING + ((l & 15) + 16 * (l >> 5)) * 16 + 8 * ((l >> 4) & 1); };
  auto AL = [&]() INL { return L0 + RING + fresh(lane) * 16; };

  const rsrc_t rt = mkrsrc(a.ws + WS_TILES, WS_TILES_BYTES);
  if (isA) {
    f32x16 acc[2][2];
    auto gemm = [&](auto xaddr, unsigned xh, unsigned xs, bool endbar) INL {
      u32x4 xf[2][4], wf[2][2];
      const unsigned l = fresh(lane), h = l >> 5, qq = (l >> 2) & 3;
      const unsigned aW = L0 + (8 * h + qq) * 512 + ((((unsigned)cw * 2 + ((l >> 4) & 1)) ^ (qq ^ (4 * h))) << 5) + 8 * (l & 3);
      auto step = [&](int ks, int hf, int xp, int domm, bool dord) INL {
        const unsigned b = aW + sl, ax = xaddr((2 * idx + ks) & 15);
        const int ph = hf ^ 1, px = hf ? xp : xp ^ 1;
#pragma unroll
        for (int i = 0; i < 4; ++i) {
          if (domm) acc[ph][i & 1] = mfma32(wf[ph][i >> 1], xf[px][i], (domm == 2 && i < 2) ? f32x16{} : acc[ph][i & 1]);
          SB();
          if (dord && i < 2) {
            wf[hf][i] = ldtr(b + i * 8192, (b ^ 128) + 2048 + i * 8192);
            if (hf == 0) { xf[xp][2 * i] = ld128(ax ^ (i * xh)); xf[xp][2 * i + 1] = ld128((ax ^ (i * xh)) + xs); }
            SB();
          }
        }
        if (dord) sl = nxt(sl);
      };
      cbar(); step(0, 0, 0, 0, true);
      rbar(); step(0, 1, 0, 2, true);
      rbar(); step(1, 0, 1, 2, true);
      rbar(); step(1, 1, 1, 1, true);
#pragma unroll 1
      for (int ks = 2; ks < 16; ks += 2) {
        rbar(); step(ks, 0, 0, 1, true);
        rbar(); step(ks, 1, 0, 1, true);
        rbar(); step(ks + 1, 0, 1, 1, true);
        rbar(); step(ks + 1, 1, 1, 1, true);
      }
      if (endbar) rbar();
      step(0, 0, 0, 1, false);
    };
    {
      const unsigned l = fresh(lane), ax = L0 + RING + (l & 31) * 256 + (((l >> 5) ^ (l & 15)) << 4);
      gemm([&](unsigned ks) INL { return (ax + (ks >> 2) * HT) ^ ((ks & 3) << 6); }, 32, 8192, true);
    }
    STAMP(1);
    {
      const unsigned l = fresh(lane);
      const unsigned aF = L0 + RING + ((cw >> 2) * 8 + (cw & 3)) * 1024 + ((l >> 4) & 1) * 4096 + (l & 15) * 16 + (l >> 5) * 8;
#pragma unroll
      for (int i = 0; i < 4; ++i)
#pragma unroll
        for (int j = 0; j < 4; ++j) {
          const f32x16& v = acc[i >> 1][i & 1];
          st64(aF + (i & 1) * 32768 + (i >> 1) * 16384 + j * 256, cvt4(f32x4{v[4 * j], v[4 * j + 1], v[4 * j + 2], v[4 * j + 3]}));
        }
    }
    {
      const unsigned aK = AK(), aV = AV();
      constexpr int PD = 6;
      u32x4 fr[8], qf[4], pf[2];
      f32x4 o[8], sa[4], sb[4], ls = Z4;
      auto rdK = [&](unsigned b, int i) INL { fr[i & 7] = ld128((b ^ ((i & 3) << 6)) + (i >> 2) * 4096); };
      auto rdV = [&](unsigned b, int i) INL { const unsigned ad = (b ^ ((i & 7) << 5)) + (i >> 3) * 8192; fr[i & 7] = ldtr(ad, ad + 4096); };
      auto one = [](float x) INL { asm volatile("" : "+v"(x)); return x; };
      float m = -INFINITY, mx;
      const u32x4 ones = {0x3f803f80u, 0x3f803f80u, 0x3f803f80u, 0x3f803f80u};
      auto phaseA = [&](f32x4 (&sn)[4], f32x4 (&sc)[4], bool domm, bool dosm, bool nextK) INL {
        const unsigned bc = sl + aK;
        if (domm) sl = nxt(sl);
        const unsigned bn = sl + (nextK ? aK : aV);
#pragma unroll
        for (int i = 0; i < 16; ++i) {
          if (domm) {
            if (i == 16 - PD) rbar();
            sn[i >> 2] = mfma(fr[i & 7], qf[i & 3], (i & 3) ? sn[i >> 2] : Z4);
            if (i + PD < 16) rdK(bc, i + PD); else if (nextK) rdK(bn, i + PD - 16); else rdV(bn, i + PD - 16);
          }
          if (dosm) {
            sc[i >> 2][i & 3] = __builtin_amdgcn_exp2f(one(fmaf(sc[i >> 2][i & 3], SL2E, -m)));
            if (i & 1) pf[i >> 3][(i >> 1) & 3] = cvtpk(sc[i >> 2][(i & 3) - 1], sc[i >> 2][i & 3]);
          }
          if (i & 1) SB();
        }
      };
      auto phaseB = [&](f32x4 (&sn)[4], bool bar, bool nextK) INL {
        const unsigned bc = sl + aV;
        sl = nxt(sl);
        const unsigned bn = sl + (nextK ? aK : aV);
#pragma unroll
        for (int i = 0; i < 16; ++i) {
          if (i == 16 - PD && bar) rbar();
          o[i & 7] = mfma(fr[i & 7], pf[i >> 3], o[i & 7]);
          if (i + PD < 16) rdV(bc, i + PD); else if (!bar) {} else if (nextK) rdK(bn, i + PD - 16); else rdV(bn, i + PD - 16);
          if ((i & 7) == 3) ls = mfma(ones, pf[i >> 3], ls);
          if (i & 1) { if (bar) mx = fmaxf(fmaxf(sn[i >> 2][(i & 3) - 1], sn[i >> 2][i & 3]), i == 1 ? -INFINITY : mx); SB(); }
        }
      };
      auto check = [&]() INL {
        if (__any(!(mx * SL2E - m <= THR))) {
          const float mn = fmaxf(m, xmax4(mx) * SL2E);
          const float alpha = __builtin_amdgcn_exp2f(m - mn);
          m = mn; ls *= alpha;
#pragma unroll
          for (int i = 0; i < 8; ++i) o[i] *= alpha;
        }
        SB();
      };
      auto rdQ = [&](int bt) INL {
        const unsigned aq = AL() + bt * 32768 + cw * 4096;
#pragma unroll
        for (int kk = 0; kk < 4; ++kk) qf[kk] = ld128(aq + kk * 1024);
      };
      auto finish = [&](int bt) INL {
        const float inv = __builtin_amdgcn_rcpf(ls[0]);
        const unsigned aF = AF() + ((bt * 2 + (cw & 1)) * 16 + (cw >> 1) * 4) * 1024;
#pragma unroll
        for (int db = 0; db < 8; ++db) { st64(aF + db * 512, cvt4(o[db] * inv)); o[db] = Z4; }
        ls = Z4;
      };
#pragma unroll
      for (int i = 0; i < 8; ++i) o[i] = Z4;
      cbar();
      rdQ(0);
#pragma unroll
      for (int i = 0; i < PD; ++i) rdK(aK + sl, i);
      phaseA(sa, sb, true, false, true);
      m = sa[0][0];
#pragma unroll
      for (int i = 0; i < 4; ++i) m = fmaxf(fmaxf(fmaxf(sa[i][0], sa[i][1]), m), fmaxf(sa[i][2], sa[i][3]));
      m = xmax4(m) * SL2E;
#pragma unroll 1
      for (int j = 0; j < 14; j += 2) {
        phaseA(sb, sa, true, true, false);
        if (j == 6) rdQ(1);
        phaseB(sb, true, true); check();
        phaseA(sa, sb, true, true, false); phaseB(sa, true, true);
        if (j == 6) { finish(0); m = xmax4(mx) * SL2E; STAMP(2); } else check();
      }
      phaseA(sb, sa, true, true, false); phaseB(sb, true, false); check();
      phaseA(sa, sb, false, true, false); phaseB(sa, false, false);
      finish(1);
      STAMP(3);
    }
    {
      const unsigned l = fresh(lane), ao = L0 + RING + ((l >> 4) & 1) * 16384 + (l >> 5) * 256 + (l & 15) * 16;
      gemm([&](unsigned ks) INL { return ao + ks * 1024; }, 512, 32768, false);
    }
    STAMP(4);
#pragma unroll
    for (int i = 0; i < 8; ++i) {
      const f32x16& v = acc[i >> 2][(i >> 1) & 1];
      const int r0 = (i & 1) * 8;
      __builtin_amdgcn_raw_buffer_store_b128(cvt8(f32x4{v[r0], v[r0 + 1], v[r0 + 2], v[r0 + 3]}, f32x4{v[r0 + 4], v[r0 + 5], v[r0 + 6], v[r0 + 7]}), rt, lane * 16,
                                             WS_TILES_A + ((2 * idx + ((i >> 1) & 1)) * 8 + xcd) * TILE_B + (((i >> 2) * 8 + cw) * 2 + (i & 1)) * 1024, 16);
    }
    asm volatile("s_waitcnt vmcnt(0)" ::: "memory");
    if (lane < 2) __hip_atomic_store((GAS u64*)(a.ws + WS_FLAG + (((2 * idx + lane) * 8 + cw) * 32 + 16 + xcd) * 8), tag, RLX);
  } else {
    unsigned aq, ax0;
    {
      const unsigned l = fresh(lane), g = l >> 4, qq = (l >> 2) & 3;
      aq = L0 + (8 * g + qq) * 256 + (((unsigned)cw ^ (qq ^ (4 * (g & 1)))) << 5) + 8 * (l & 3);
      ax0 = AK();
    }
    const unsigned al = AL() + poff, aF = AF() + cw * 512 + poff;
    f32x4 qa[2];
    u32x4 wf[2][2], xf[2][4];
    auto qrd = [&](int st, int p) INL {
      const unsigned ii = (st + bB) & 7, b = aq + sl, ax = (ax0 + (RING + 0u + (ii >> 1) * 8192)) ^ ((ii & 1) << 7);
#pragma unroll
      for (int ks = 0; ks < 2; ++ks) wf[p][ks] = ldtr(b + ks * 8192, (b ^ 128) + 1024 + ks * 8192);
#pragma unroll
      for (int i = 0; i < 4; ++i) xf[p][i] = ld128((ax ^ ((i >> 1) << 6)) + (i & 1) * 4096);
      sl = nxt(sl);
      SB();
    };
    auto qmm = [&](int p) INL {
#pragma unroll
      for (int i = 0; i < 4; ++i) qa[i & 1] = mfma(wf[p][i >> 1], xf[p][i], qa[i & 1]);
      SB();
    };
    auto qrest = [&]() INL {
#pragma unroll 1
      for (int st = 1; st < 7; st += 2) {
        rbar(); qrd(st, 1); qmm(0);
        rbar(); qrd(st + 1, 0); qmm(1);
      }
      rbar(); qrd(7, 1); qmm(0);
    };
    auto qwr = [&]() INL {
#pragma unroll
      for (int qb = 0; qb < 2; ++qb) st64(aF + Q_OFF + qb * 4096, cvt4(qa[qb]));
      SB();
    };
    qa[0] = Z4; qa[1] = Z4;
    rbar(); qrd(0, 0); qrest(); qmm(1); qwr();
    STAMP(8);
    cbar();
    f32x4 oa[4][2];
#pragma unroll
    for (int nb = 0; nb < 4; ++nb) { oa[nb][0] = Z4; oa[nb][1] = Z4; }
#pragma unroll 1
    for (int u = 0; u < nu; ++u) {
      f32x4 s[8];
      float mx = -INFINITY;
      const unsigned aS = L0 + RING + poff + ST_OFF + (fresh(lane) & 15) * 16;
      const unsigned aP = al + ((cw >> 2) * 2048 + (cw & 1) * 1024 + ((cw >> 1) & 1) * 8);
      const unsigned aV = AV() ^ ((unsigned)cw << 5), aw = AW();
      {
        u32x4 qf[4], kf[2][4];
        const unsigned aK = ax0 + (cw >> 1) * 4096;
        auto kmm = [&](int i) INL {
          s[i] = Z4;
#pragma unroll
          for (int kk = 0; kk < 4; ++kk) s[i] = mfma(kf[i & 1][kk], qf[kk], s[i]);
          if (i > 0) mx = fmaxf(fmaxf(fmaxf(s[i - 1][0], s[i - 1][1]), mx), fmaxf(s[i - 1][2], s[i - 1][3]));
          SB();
        };
#pragma unroll
        for (int i = 0; i < 8; ++i) {
          if (i > 0 || u > 0) rbar();
          const unsigned b = aK + sl;
#pragma unroll
          for (int kk = 0; kk < 4; ++kk) kf[i & 1][kk] = ld128(b ^ (kk << 6));
          if (i == 0) {
#pragma unroll
            for (int kk = 0; kk < 4; ++kk) qf[kk] = ld128(al + (Q_OFF + (cw & 1) * 4096) + kk * 1024);
          }
          sl = nxt(sl);
          SB();
          if (i > 0 && i != 6) kmm(i - 1);
          if (i == 5) {
            kmm(5);
            if (early && u > 0) {
#pragma unroll
              for (int t = 0; t <= 8; ++t) {
                if (t < 8) {
                  rbar();
                  const unsigned bw = aw + sl;
#pragma unroll
                  for (int nb = 0; nb < 2; ++nb) wf[t & 1][nb] = ldtr(bw ^ (nb << 5), (bw ^ (nb << 5) ^ 128) + 2048);
                  if (!(t & 1)) {
#pragma unroll
                    for (int qb = 0; qb < 2; ++qb) xf[(t >> 1) & 1][qb] = ld128(al + (((t >> 1) + bB) & 3) * 1024 + qb * 4096);
                  }
                  sl = nxt(sl);
                  SB();
                }
                if (t > 0) {
                  const int p = t - 1;
#pragma unroll
                  for (int j = 0; j < 4; ++j) oa[(p & 1) * 2 + (j >> 1)][j & 1] = mfma(wf[p & 1][j >> 1], xf[(p >> 1) & 1][j & 1], oa[(p & 1) * 2 + (j >> 1)][j & 1]);
                  SB();
                }
              }
            }
            if (early && u + 1 < nu) {
              qa[0] = Z4; qa[1] = Z4;
              rbar(); qrd(0, 0); qrest(); qmm(1); qwr();
            }
          }
        }
        kmm(7);
      }
      if (u == 0) STAMP(1);
      mx = xmax4(fmaxf(fmaxf(fmaxf(s[7][0], s[7][1]), mx), fmaxf(s[7][2], s[7][3])));
      if (lane < 16) *(LAS float*)(uintptr_t)(aS + (cw & 1) * 256 + (cw >> 1) * 4) = mx;
      cbar();
      const f32x4 m0 = ldf4(aS + (cw & 1) * 256);
      const float mn = SL2E * fmaxf(fmaxf(m0[0], m0[1]), fmaxf(m0[2], m0[3]));
      f32x4 ps = Z4;
      auto pexp = [&](int i) INL {
        const f32x4 e = s[i] * SL2E - mn;
        f32x4 p;
#pragma unroll
        for (int r = 0; r < 4; ++r) p[r] = __builtin_amdgcn_exp2f(e[r]);
        ps += p;
        st64(aP + (i & 3) * 4096, cvt4(p));
        SB();
      };
      pexp(0); pexp(1);
      f32x4 o[2] = {Z4, Z4};
      u32x4 vf[2][2], pf[2][4];
      auto vrd = [&](int i) INL {
        const unsigned b = aV + sl;
#pragma unroll
        for (int ks = 0; ks < 2; ++ks) vf[i & 1][ks] = ldtr(b + ks * 8192, b + ks * 8192 + 4096);
#pragma unroll
        for (int j = 0; j < 4; ++j) pf[i & 1][j] = ld128(al + ((i & 3) * 4096 + j * 1024));
        sl = nxt(sl);
        SB();
      };
      auto vmm = [&](int i) INL {
#pragma unroll
        for (int j = 0; j < 4; ++j) o[j & 1] = mfma(vf[i & 1][j >> 1], pf[i & 1][j], o[j & 1]);
        SB();
      };
#pragma unroll
      for (int i = 0; i < 6; ++i) {
        if (i == 0) cbar(); else { SB(); asm volatile("s_waitcnt lgkmcnt(9)\n\ts_barrier" ::: "memory"); SB(); }
        vrd(i);
        if (i > 0) vmm(i - 1);
        pexp(i + 2);
      }
      {
        const float pt = xsum4((ps[0] + ps[1]) + (ps[2] + ps[3]));
        if (lane < 16) *(LAS float*)(uintptr_t)(aS + 512 + (cw & 1) * 256 + (cw >> 1) * 4) = pt;
        SB();
      }
      auto vtail = [&]() INL {
        cbar(); vrd(6);
        float inv[2];
#pragma unroll
        for (int qb = 0; qb < 2; ++qb) { const f32x4 l0 = ldf4(aS + 512 + qb * 256); inv[qb] = __builtin_amdgcn_rcpf((l0[0] + l0[1]) + (l0[2] + l0[3])); }
        vmm(6);
        rbar(); vrd(7); vmm(7);
#pragma unroll
        for (int qb = 0; qb < 2; ++qb) st64(aF + qb * 4096, cvt4(o[qb] * inv[qb]));
        cbar();
      };
      int lim = early ? nu - 1 : 0;
      if (u < lim) { vmm(5); vtail(); }
      asm volatile("" : "+s"(lim));
      if (u >= lim) {
        u32x4 wo[4][4];
        auto wrd = [&](int i) INL {
          const unsigned b = aw + sl;
#pragma unroll
          for (int nb = 0; nb < 2; ++nb) wo[i >> 1][(i & 1) * 2 + nb] = ldtr(b ^ (nb << 5), (b ^ (nb << 5) ^ 128) + 2048);
          sl = nxt(sl);
          SB();
        };
        if (u + 1 < nu) {
          qa[0] = Z4; qa[1] = Z4;
          rbar(); qrd(0, 0); vmm(5);
          qrest();
          rbar(); wrd(0); qmm(1);
          rbar(); wrd(1); qwr();
        } else {
          rbar(); wrd(0); vmm(5);
          rbar(); wrd(1);
        }
#pragma unroll
        for (int i = 2; i < 8; ++i) {
          SB(); asm volatile("s_waitcnt lgkmcnt(4)\n\ts_barrier" ::: "memory"); SB();
          wrd(i);
        }
        vtail();
        u32x4 of[2][4];
#pragma unroll
        for (int qb = 0; qb < 2; ++qb)
#pragma unroll
          for (int kk = 0; kk < 4; ++kk) of[qb][kk] = ld128(al + ((kk + bB) & 3) * 1024 + qb * 4096);
        SB();
#pragma unroll
        for (int qb = 0; qb < 2; ++qb)
#pragma unroll
          for (int kk = 0; kk < 4; ++kk)
#pragma unroll
            for (int nb = 0; nb < 4; ++nb) oa[nb][qb] = mfma(wo[kk][nb], of[qb][kk], oa[nb][qb]);
      }
      STAMP(2 + u);
    }
#pragma unroll
    for (int i = 0; i < 4; ++i)
      __builtin_amdgcn_raw_buffer_store_b128(cvt8(oa[(i & 1) * 2][i >> 1], oa[(i & 1) * 2 + 1][i >> 1]), rt, lane * 16, (bB * 12 + slotB) * TILE_B + (((i & 1) * 8 + cw) * 2 + (i >> 1)) * 1024, 16);
    asm volatile("s_waitcnt vmcnt(0)" ::: "memory");
    if (lane == 0) __hip_atomic_store((GAS u64*)(a.ws + WS_FLAG + ((bB * 8 + cw) * 32 + slotB) * 8), tag, RLX);
  }
  STAMP(5);

  {
    const int rb = blockIdx.x >> 4, pj = blockIdx.x & 15, pc = pj * 2 + ((cw >> 1) & 1), hb = cw & 1;
    const bool wB = cw < 4;
    const GAS u64* fp = (const GAS u64*)(a.ws + WS_FLAG + ((rb * 8 + (pj & 7)) * 32 + (wB ? (lane < 12 ? lane : 0) : 16 + (lane & 7))) * 8);
    for (int spin = 0; spin < NSPIN; ++spin) {
      if (__all(__hip_atomic_load(fp, RLX) == tag)) break;
      __builtin_amdgcn_s_sleep(SLP);
    }
    __builtin_amdgcn_fence(__ATOMIC_ACQUIRE, "wavefront");
    STAMP(6);
    const unsigned so = (wB ? rb * 12 * TILE_B : WS_TILES_A + rb * 8 * TILE_B) + pc * 1024 + hb * 512;
    const unsigned vo = (lane >> 5) * TILE_B + (lane & 31) * 16;
    u32x4 t[6];
#pragma unroll
    for (int i = 0; i < 6; ++i) t[i] = __builtin_amdgcn_raw_buffer_load_b128(rt, (i < 4 || wB) ? vo : 0x7fffff00u, so + i * 2 * TILE_B, 16);
    f32x4 s0 = Z4, s1 = Z4;
#pragma unroll
    for (int i = 0; i < 6; ++i) { s0 += bf4(t[i][0], t[i][1]); s1 += bf4(t[i][2], t[i][3]); }
    f32x4 r;
#pragma unroll
    for (int j = 0; j < 4; ++j) {
      const auto x = __builtin_amdgcn_permlane32_swap(__float_as_uint(s0[j]), __float_as_uint(s1[j]), false, false);
      r[j] = __uint_as_float(x[0]) + __uint_as_float(x[1]);
    }
    const unsigned sl_ = hb * 32 + (lane & 31);
    const unsigned row = wB ? (pc & 1) * 16 + (sl_ & 15) : (lane & 31);
    const unsigned col = wB ? 512 + 16 * (lane >> 5) + 4 * (sl_ >> 4) : 16 * (pc & 1) + 8 * (lane >> 5) + 4 * hb;
    *(GAS u32x2*)(a.out + ((size_t)(rb * SQ + row) * 1024 + 32 * (pc >> 1) + col) * 2) = cvt4(r);
  }
  STAMP(7);
}

extern "C" void kernel_launch(void* const* d_in, const int* in_sizes, int n_in, void* d_out, int out_size, void* d_ws, size_t ws_size,
                              hipStream_t stream) {
  Args a;
  for (int br = 0; br < 2; ++br) {
    a.x[br] = d_in[5 * br + 0];
    a.k[br] = d_in[5 * br + 1];
    a.v[br] = d_in[5 * br + 2];
    a.wq[br] = d_in[5 * br + 3];
    a.wo[br] = d_in[5 * br + 4];
  }
  a.ws = (char*)d_ws;
  a.out = (char*)d_out;
  (void)hipFuncSetAttribute((const void*)fused_k, hipFuncAttributeMaxDynamicSharedMemorySize, LDS_BYTES);
  fused_k<<<256, 768, LDS_BYTES, stream>>>(a);
}
```

```cpp
#include <hip/hip_runtime.h>
#include <stdint.h>

#define LAS __attribute__((address_space(3)))
#define GAS __attribute__((address_space(1)))
typedef unsigned u32x4 __attribute__((ext_vector_type(4)));
typedef unsigned u32x2 __attribute__((ext_vector_type(2)));
typedef float f32x4 __attribute__((ext_vector_type(4)));
typedef float f32x2 __attribute__((ext_vector_type(2)));
typedef short bf16x8 __attribute__((ext_vector_type(8)));
typedef short s16x4 __attribute__((ext_vector_type(4)));
typedef __bf16 bf16x2 __attribute__((ext_vector_type(2)));
typedef __amdgpu_buffer_rsrc_t rsrc_t;
typedef unsigned long long u64;

constexpr int NB = 16, SQ = 32, DM = 512, NH = 32, SK = 512, DH = 128;
constexpr float SL2E = 0.088388347648318447f * 1.4426950408889634f;
constexpr unsigned HT = 16384, RING = 6 * HT;
constexpr unsigned Q_OFF = 16384, ST_OFF = Q_OFF + 8192;
constexpr unsigned LDS_BYTES = 160 * 1024;
constexpr int NLD = 4;
constexpr unsigned KVB = SK * DH * 2;
constexpr unsigned WS_FLAG = 4096, WS_TILES = 1u << 20, TILE_B = 32768;
constexpr unsigned WS_TILES_A = 192 * TILE_B, WS_TILES_BYTES = 320 * TILE_B;

struct Args {
  const void* x[2]; const void* k[2]; const void* v[2]; const void* wq[2]; const void* wo[2];
  char* ws; char* out;
};

#define SB() __builtin_amdgcn_sched_barrier(0)
#define INL __attribute__((always_inline))
#define TAGC 0x9E3779B97F4A7C15ull
#define RLX __ATOMIC_RELAXED, __HIP_MEMORY_SCOPE_AGENT
#define Z4 f32x4{0.f, 0.f, 0.f, 0.f}
__device__ __forceinline__ rsrc_t mkrsrc(const void* p, unsigned bytes) { return __builtin_amdgcn_make_buffer_rsrc((void*)p, 0, bytes, 0x00020000); }
template <int AUX, int N> __device__ __forceinline__ void dma(rsrc_t r, unsigned lds, const unsigned (&v)[4], unsigned soff) {
  __builtin_amdgcn_raw_ptr_buffer_load_lds(r, (LAS void*)(uintptr_t)lds, 16, v[0], soff, 0, AUX);
  __builtin_amdgcn_raw_ptr_buffer_load_lds(r, (LAS void*)(uintptr_t)lds, 16, v[1], soff, 1024, AUX);
  if (N > 2) {
    __builtin_amdgcn_raw_ptr_buffer_load_lds(r, (LAS void*)(uintptr_t)lds, 16, v[2], soff, 2048, AUX);
    __builtin_amdgcn_raw_ptr_buffer_load_lds(r, (LAS void*)(uintptr_t)lds, 16, v[3], soff, 3072, AUX);
  }
}
template <int N> __device__ __forceinline__ void waitb() { asm volatile("s_waitcnt vmcnt(%0)\n\ts_barrier" :: "i"(N) : "memory"); }
#define WAITB(N) waitb<N>()
__device__ __forceinline__ void lbar() { asm volatile("s_barrier" ::: "memory"); }
__device__ __forceinline__ void rbar() { SB(); asm volatile("s_barrier" ::: "memory"); SB(); }
__device__ __forceinline__ void cbar() { SB(); asm volatile("s_waitcnt lgkmcnt(0)\n\ts_barrier" ::: "memory"); SB(); }
__device__ __forceinline__ void cbar_vm() { SB(); asm volatile("s_waitcnt vmcnt(0) lgkmcnt(0)\n\ts_barrier" ::: "memory"); SB(); }
__device__ __forceinline__ f32x4 mfma(u32x4 a, u32x4 b, f32x4 c) {
  return __builtin_amdgcn_mfma_f32_16x16x32_bf16(__builtin_bit_cast(bf16x8, a), __builtin_bit_cast(bf16x8, b), c, 0, 0, 0);
}
typedef float f32x16 __attribute__((ext_vector_type(16)));
__device__ __forceinline__ f32x16 mfma32(u32x4 a, u32x4 b, f32x16 c) {
  return __builtin_amdgcn_mfma_f32_32x32x16_bf16(__builtin_bit_cast(bf16x8, a), __builtin_bit_cast(bf16x8, b), c, 0, 0, 0);
}
__device__ __forceinline__ unsigned cvtpk(float lo, float hi) { return __builtin_bit_cast(unsigned, __builtin_convertvector(f32x2{lo, hi}, bf16x2)); }
__device__ __forceinline__ u32x2 cvt4(f32x4 v) { return u32x2{cvtpk(v[0], v[1]), cvtpk(v[2], v[3])}; }
__device__ __forceinline__ u32x4 cvt8(f32x4 a, f32x4 b) { return u32x4{cvtpk(a[0], a[1]), cvtpk(a[2], a[3]), cvtpk(b[0], b[1]), cvtpk(b[2], b[3])}; }
__device__ __forceinline__ f32x4 bf4(unsigned lo, unsigned hi) { return f32x4{__uint_as_float(lo << 16), __uint_as_float(lo & 0xffff0000u), __uint_as_float(hi << 16), __uint_as_float(hi & 0xffff0000u)}; }
__device__ __forceinline__ u32x4 ld128(unsigned a) { return *(const LAS u32x4*)(uintptr_t)a; }
__device__ __forceinline__ f32x4 ldf4(unsigned a) { return *(const LAS f32x4*)(uintptr_t)a; }
__device__ __forceinline__ void st64(unsigned a, u32x2 v) { *(LAS u32x2*)(uintptr_t)a = v; }
__device__ __forceinline__ u32x4 ldtr(unsigned a0, unsigned a1) {
  const u32x2 lo = __builtin_bit_cast(u32x2, __builtin_amdgcn_ds_read_tr16_b64_v4i16((LAS s16x4*)(uintptr_t)a0));
  const u32x2 hi = __builtin_bit_cast(u32x2, __builtin_amdgcn_ds_read_tr16_b64_v4i16((LAS s16x4*)(uintptr_t)a1));
  return u32x4{lo[0], lo[1], hi[0], hi[1]};
}
__device__ __forceinline__ float xmax4(float x) {
  auto s = __builtin_amdgcn_permlane16_swap(__float_as_uint(x), __float_as_uint(x), false, false);
  x = fmaxf(__uint_as_float(s[0]), __uint_as_float(s[1]));
  auto t = __builtin_amdgcn_permlane32_swap(__float_as_uint(x), __float_as_uint(x), false, false);
  return fmaxf(__uint_as_float(t[0]), __uint_as_float(t[1]));
}
__device__ __forceinline__ float xsum4(float x) {
  auto s = __builtin_amdgcn_permlane16_swap(__float_as_uint(x), __float_as_uint(x), false, false);
  x = __uint_as_float(s[0]) + __uint_as_float(s[1]);
  auto t = __builtin_amdgcn_permlane32_swap(__float_as_uint(x), __float_as_uint(x), false, false);
  return __uint_as_float(t[0]) + __uint_as_float(t[1]);
}
__device__ __forceinline__ unsigned nxt(unsigned s) { s += HT; return s == RING ? 0u : s; }
__device__ __forceinline__ unsigned fresh(unsigned x) { asm volatile("" : "+v"(x)); return x; }
#define STAMP(i) do {} while (0)
#define KVMASK 0xffffffffu
#define KVAUX 2
#define EXP 2
#define THR 8.f
#define KROT 0
#define SLP 2
#define PACES 14
#define YSEL (idx & 1)
#define PVB 6
#define PACE2 0
#define PF 1
#define NSPIN (1 << 16)

#define TMARK(k) do {} while (0)
#define WGI (idx * 8 + xcd)

__global__ void __launch_bounds__(768) fused_k(Args a) {
  extern __shared__ __attribute__((aligned(16))) char smem[];
  const unsigned lane = threadIdx.x & 63;
  const int w = __builtin_amdgcn_readfirstlane(threadIdx.x >> 6);
  const int xcd = blockIdx.x & 7, pi = blockIdx.x >> 3;
  const int idx = (pi & 1) ? 8 + (pi >> 1) : (pi < 16 ? pi >> 1 : 16 + (pi >> 1));
  const bool isA = idx < 8;
  const int nu = idx < 24 ? 3 : 2;
  const int bB = nu == 3 ? idx - 8 : (xcd & 1) * 8 + (idx - 24);
  const int slotB = nu == 3 ? xcd : 8 + (xcd >> 1);
  auto headB = [&](int u) INL { int x = u + bB % 3; x = x >= 3 ? x - 3 : x; return nu == 3 ? 4 * xcd + x : 4 * (xcd ^ ((u ^ bB) & 1)) + 3; };
  const bool early = YSEL;
  constexpr unsigned poff = 32768;
  const unsigned L0 = (unsigned)(uintptr_t)(LAS char*)smem;

  if (__builtin_expect(w < NLD, 1)) {
    const unsigned l = w;
    const rsrc_t rx = mkrsrc(isA ? a.x[0] : a.x[1], NB * SQ * DM * 2), rq = mkrsrc(isA ? a.wq[0] : a.wq[1], DM * NH * DH * 2);
    const unsigned s16 = lane & 15, e4 = lane >> 4, e2 = lane >> 5;
    auto offK = [&](int q, unsigned rs) INL { return (16 * l + e4) * rs + q * (4 * rs - 1024) + ((s16 ^ (4 * q + e4)) << 4); };
    auto offT = [&](int q, unsigned rs) INL { return (16 * l + e4) * rs + q * (4 * rs - 1024) + ((s16 ^ ((e4 ^ (((q ^ (q >> 1)) & 1) * 4)) << 1)) << 4); };
    auto offW = [&](int q, unsigned rs) INL { return (8 * l + e2) * rs + q * (2 * rs - 1024) + (((lane & 31) * 16) ^ (((2 * q + e2) ^ (4 * (l & 1))) * 32)); };
    const unsigned pl = L0 + l * 4096;
    unsigned sl = 0;
    auto wt = [&](rsrc_t r, const unsigned (&v)[4], unsigned so) INL { dma<0, 4>(r, pl + sl, v, so); sl = nxt(sl); };
    auto kvt = [&](rsrc_t r, const unsigned (&v)[4], unsigned so) INL {
      if (PACES && nu == 2) __builtin_amdgcn_s_sleep(PACES);
      dma<2, 4>(r, pl + sl, v, so); sl = nxt(sl);
    };
    auto kvt0 = [&](rsrc_t r, const unsigned (&v)[4], unsigned so) INL {
      if (PACES && nu == 2) __builtin_amdgcn_s_sleep(PACES);
      dma<0, 4>(r, pl + sl, v, so); sl = nxt(sl);
    };
    unsigned vX[4], vQ[4], vK[4], vV[4], vO[4];
    auto late = [&]() INL {
      __builtin_amdgcn_s_setprio(3);
      SB();
#pragma unroll
      for (int q = 0; q < 4; ++q) { vK[q] = fresh(offK(q, 256)); vV[q] = fresh(offT(q, 256)); vO[q] = fresh(offW(q, 1024)); }
      asm volatile("" :: "s"(a.k[0]), "s"(a.k[1]), "s"(a.v[0]), "s"(a.v[1]), "s"(a.wo[0]), "s"(a.wo[1]));
      SB();
    };
    if (__builtin_expect(!isA, 1)) {
#pragma unroll
      for (int q = 0; q < 4; ++q) { vX[q] = (8 * l + e4) * 1024 + q * 3072 + ((s16 ^ (8 * (l & 1) + 4 * q + e4)) << 4); vQ[q] = offT(q, 8192); }
      auto xt = [&](int i) INL { const unsigned c4 = ((bB >> 1) + i) & 3; dma<0, 2>(rx, L0 + RING + c4 * 8192 + l * 2048, vX, (unsigned)bB * (SQ * DM * 2) + c4 * 256); };
      auto qt = [&](int h, int i) INL { wt(rq, vQ, ((i + bB) & 7) * (64 * 8192) + h * 256); };
      int h = headB(0);
      TMARK(126);
      xt(0); qt(h, 0); xt(1); qt(h, 1); xt(2); qt(h, 2); xt(3); qt(h, 3);
      TMARK(127);
      late();
      WAITB(18); qt(h, 4);
      WAITB(16); qt(h, 5);
      WAITB(14); qt(h, 6);
      WAITB(12); qt(h, 7);
      {
        const rsrc_t rk = mkrsrc(a.k[1], NB * NH * KVB), rv = mkrsrc(a.v[1], NB * NH * KVB), ro = mkrsrc(a.wo[1], DM * NH * DH * 2);
        unsigned kvo = (unsigned)(bB * NH + h) * KVB;
        int hp = h;
        auto ot = [&](int hh, int i) INL { wt(ro, vO, hh * (128 * 1024) + (((i >> 1) + bB) & 3) * (32 * 1024) + (i & 1) * 512); };
#pragma unroll
        for (int i = 0; i < 6; ++i) { WAITB(12); kvt0(rk, vK, kvo + i * HT); }
#pragma unroll 1
        for (int u = 0;; ++u) {
          const int hn = headB(u + 1);
          if (early && u > 0) {
#pragma unroll
            for (int i = 0; i < 8; ++i) { WAITB(12); ot(hp, i); }
          }
          if (early && u + 1 < nu) {
#pragma unroll
            for (int i = 0; i < 8; ++i) { WAITB(12); qt(hn, i); }
          }
          WAITB(12); kvt0(rk, vK, kvo + 6 * HT);
          WAITB(12); kvt0(rk, vK, kvo + 7 * HT);
#pragma unroll
          for (int i = 0; i < 3; ++i) { WAITB(12); kvt(rv, vV, kvo + i * HT); }
          WAITB(12); lbar();
#pragma unroll
          for (int i = 3; i < 6; ++i) kvt(rv, vV, kvo + i * HT);
          WAITB(20); WAITB(16);
          if (!early && u + 1 < nu) {
#pragma unroll
            for (int i = 0; i < 8; ++i) { WAITB(12); qt(hn, i); }
          }
          if (!early || u + 1 == nu) {
#pragma unroll
            for (int i = 0; i < 8; ++i) { WAITB(12); ot(h, i); }
          }
          WAITB(12); kvt(rv, vV, kvo + 6 * HT);
          WAITB(12); kvt(rv, vV, kvo + 7 * HT);
          if (u + 1 == nu) break;
          hp = h; h = hn;
          kvo = (unsigned)(bB * NH + h) * KVB;
#pragma unroll
          for (int i = 0; i < 3; ++i) { WAITB(12); kvt0(rk, vK, kvo + i * HT); }
          WAITB(12); lbar();
#pragma unroll
          for (int i = 3; i < 6; ++i) kvt0(rk, vK, kvo + i * HT);
          WAITB(20); WAITB(16);
        }
      }
    } else {
#pragma unroll
      for (int q = 0; q < 4; ++q) { vX[q] = offK(q, 1024); vQ[q] = offW(q, 8192); }
      auto xt = [&](int i) INL { const unsigned c4 = ((idx >> 1) + i) & 3; dma<0, 4>(rx, pl + RING + c4 * HT, vX, (unsigned)idx * (2 * SQ * DM * 2) + c4 * 256); };
      auto qt = [&](int i) INL { wt(rq, vQ, ((2 * idx + (i >> 1)) & 15) * (32 * 8192) + xcd * 1024 + (i & 1) * 512); };
      TMARK(126);
      xt(0); qt(0); qt(1); qt(2); xt(1); qt(3);
      TMARK(127);
      late();
      const rsrc_t rk = mkrsrc(a.k[0], NB * 8 * KVB), rv = mkrsrc(a.v[0], NB * 8 * KVB), ro = mkrsrc(a.wo[0], DM * NH * DH * 2);
      WAITB(16); qt(4); xt(2);
      WAITB(20); qt(5); xt(3);
      WAITB(12); qt(6);
      WAITB(12); qt(7);
#pragma unroll 4
      for (int i = 8; i < 32; ++i) { WAITB(12); qt(i); }
      auto kvo = [&](int j) INL { return (unsigned)((2 * idx + (j >> 3)) * 8 + xcd) * KVB + (j & 7) * HT; };
      WAITB(12); kvt0(rk, vK, kvo(0));
      WAITB(12); kvt0(rk, vK, kvo(1));
      WAITB(12); kvt(rv, vV, kvo(0));
      WAITB(12); lbar(); kvt0(rk, vK, kvo(2));
      WAITB(12); kvt(rv, vV, kvo(1));
#pragma unroll 1
      for (int j = 2; j < 15; ++j) {
        WAITB(12); kvt0(rk, vK, kvo(j + 1));
        WAITB(12); kvt(rv, vV, kvo(j));
      }
      WAITB(12); kvt(rv, vV, kvo(15));
#pragma unroll 4
      for (int i = 0; i < 32; ++i) { WAITB(12); wt(ro, vO, xcd * (512 * 1024) + ((2 * idx + (i >> 1)) & 15) * (32 * 1024) + (i & 1) * 512); }
    }
    WAITB(12); WAITB(8); WAITB(4); WAITB(0);
    if (blockIdx.x == 240 && w == 0) {
      lbar();
      const u64 tag = __hip_atomic_load((const GAS u64*)a.ws, RLX) + TAGC;
      const unsigned sidx = lane & 31, su = (sidx < 12 || (sidx >= 16 && sidx < 24)) ? sidx : 0;
      for (int spin = 0; spin < NSPIN; ++spin) {
        bool ok = true;
#pragma unroll
        for (int i = 0; i < 8; ++i) ok &= __hip_atomic_load((const GAS u64*)(a.ws + WS_FLAG + ((i * 2 + (lane >> 5)) * 256 + su) * 8), RLX) == tag;
        if (__all(ok)) break;
        __builtin_amdgcn_s_sleep(4);
      }
      if (lane == 0) __hip_atomic_store((GAS u64*)a.ws, tag, RLX);
    }
    return;
  }

  const int cw = w - NLD;
  STAMP(0);
  if (PF && nu == 2) {
    const GAS char* p = (const GAS char*)a.wq[0] + (idx - 24) * (64 * 8192) + xcd * 1024 + (size_t)(cw * 8 + (lane >> 4)) * 8192 + (lane & 15) * 64;
    const unsigned t0 = *(const GAS unsigned*)p, t1 = *(const GAS unsigned*)(p + 4 * 8192);
    asm volatile("" :: "v"(t0), "v"(t1));
  }
  const u64 ep = __hip_atomic_load((const GAS u64*)a.ws, RLX);
  const u64 tag = (((u64)(unsigned)__builtin_amdgcn_readfirstlane((unsigned)(ep >> 32)) << 32) | (unsigned)__builtin_amdgcn_readfirstlane((unsigned)ep)) + TAGC;
  unsigned sl = 0;
  auto AK = [&]() INL { const unsigned l = fresh(lane), c = l & 15, g = l >> 4; return L0 + c * 256 + ((g ^ c) << 4); };
  auto AV = [&]() INL { const unsigned l = fresh(lane), g = l >> 4, qq = (l >> 2) & 3;
                    return L0 + (4 * g + qq) * 256 + (((4 * (g & 1) + qq) ^ (4 * (g >> 1))) << 5) + 8 * (l & 3); };
  auto AW = [&]() INL { const unsigned l = fresh(lane), g = l >> 4, qq = (l >> 2) & 3;
                    return L0 + (8 * g + qq) * 512 + ((((unsigned)cw * 2) ^ (qq ^ (4 * (g & 1)))) << 5) + 8 * (l & 3); };
  auto AF = [&]() INL { const unsigned l = fresh(lane); return L0 + RING + ((l & 15) + 16 * (l >> 5)) * 16 + 8 * ((l >> 4) & 1); };
  auto AL = [&]() INL { return L0 + RING + fresh(lane) * 16; };

  const rsrc_t rt = mkrsrc(a.ws + WS_TILES, WS_TILES_BYTES);
  if (isA) {
    f32x16 acc[2][2];
    auto gemm = [&](auto xaddr, unsigned xh, unsigned xs, bool endbar) INL {
      u32x4 xf[2][4], wf[2][2];
      const unsigned l = fresh(lane), h = l >> 5, qq = (l >> 2) & 3;
      const unsigned aW = L0 + (8 * h + qq) * 512 + ((((unsigned)cw * 2 + ((l >> 4) & 1)) ^ (qq ^ (4 * h))) << 5) + 8 * (l & 3);
      auto step = [&](int ks, int hf, int xp, int domm, bool dord) INL {
        const unsigned b = aW + sl, ax = xaddr((2 * idx + ks) & 15);
        const int ph = hf ^ 1, px = hf ? xp : xp ^ 1;
#pragma unroll
        for (int i = 0; i < 4; ++i) {
          if (domm) acc[ph][i & 1] = mfma32(wf[ph][i >> 1], xf[px][i], (domm == 2 && i < 2) ? f32x16{} : acc[ph][i & 1]);
          SB();
          if (dord && i < 2) {
            wf[hf][i] = ldtr(b + i * 8192, (b ^ 128) + 2048 + i * 8192);
            if (hf == 0) { xf[xp][2 * i] = ld128(ax ^ (i * xh)); xf[xp][2 * i + 1] = ld128((ax ^ (i * xh)) + xs); }
            SB();
          }
        }
        if (dord) sl = nxt(sl);
      };
      cbar(); step(0, 0, 0, 0, true);
      rbar(); step(0, 1, 0, 2, true);
      rbar(); step(1, 0, 1, 2, true);
      rbar(); step(1, 1, 1, 1, true);
#pragma unroll 1
      for (int ks = 2; ks < 16; ks += 2) {
        rbar(); step(ks, 0, 0, 1, true);
        rbar(); step(ks, 1, 0, 1, true);
        rbar(); step(ks + 1, 0, 1, 1, true);
        rbar(); step(ks + 1, 1, 1, 1, true);
      }
      if (endbar) rbar();
      step(0, 0, 0, 1, false);
    };
    {
      const unsigned l = fresh(lane), ax = L0 + RING + (l & 31) * 256 + (((l >> 5) ^ (l & 15)) << 4);
      gemm([&](unsigned ks) INL { return (ax + (ks >> 2) * HT) ^ ((ks & 3) << 6); }, 32, 8192, true);
    }
    STAMP(1);
    {
      const unsigned l = fresh(lane);
      const unsigned aF = L0 + RING + ((cw >> 2) * 8 + (cw & 3)) * 1024 + ((l >> 4) & 1) * 4096 + (l & 15) * 16 + (l >> 5) * 8;
#pragma unroll
      for (int i = 0; i < 4; ++i)
#pragma unroll
        for (int j = 0; j < 4; ++j) {
          const f32x16& v = acc[i >> 1][i & 1];
          st64(aF + (i & 1) * 32768 + (i >> 1) * 16384 + j * 256, cvt4(f32x4{v[4 * j], v[4 * j + 1], v[4 * j + 2], v[4 * j + 3]}));
        }
    }
    {
      const unsigned aK = AK(), aV = AV();
      constexpr int PD = 6;
      u32x4 fr[8], qf[4], pf[2];
      f32x4 o[8], sa[4], sb[4], ls = Z4;
      auto rdK = [&](unsigned b, int i) INL { fr[i & 7] = ld128((b ^ ((i & 3) << 6)) + (i >> 2) * 4096); };
      auto rdV = [&](unsigned b, int i) INL { const unsigned ad = (b ^ ((i & 7) << 5)) + (i >> 3) * 8192; fr[i & 7] = ldtr(ad, ad + 4096); };
      auto one = [](float x) INL { asm volatile("" : "+v"(x)); return x; };
      float m = -INFINITY, mx;
      const u32x4 ones = {0x3f803f80u, 0x3f803f80u, 0x3f803f80u, 0x3f803f80u};
      auto phaseA = [&](f32x4 (&sn)[4], f32x4 (&sc)[4], bool domm, bool dosm, bool nextK) INL {
        const unsigned bc = sl + aK;
        if (domm) sl = nxt(sl);
        const unsigned bn = sl + (nextK ? aK : aV);
#pragma unroll
        for (int i = 0; i < 16; ++i) {
          if (domm) {
            if (i == 16 - PD) rbar();
            sn[i >> 2] = mfma(fr[i & 7], qf[i & 3], (i & 3) ? sn[i >> 2] : Z4);
            if (i + PD < 16) rdK(bc, i + PD); else if (nextK) rdK(bn, i + PD - 16); else rdV(bn, i + PD - 16);
          }
          if (dosm) {
            sc[i >> 2][i & 3] = __builtin_amdgcn_exp2f(one(fmaf(sc[i >> 2][i & 3], SL2E, -m)));
            if (i & 1) pf[i >> 3][(i >> 1) & 3] = cvtpk(sc[i >> 2][(i & 3) - 1], sc[i >> 2][i & 3]);
          }
          if (i & 1) SB();
        }
      };
      auto phaseB = [&](f32x4 (&sn)[4], bool bar, bool nextK) INL {
        const unsigned bc = sl + aV;
        sl = nxt(sl);
        const unsigned bn = sl + (nextK ? aK : aV);
#pragma unroll
        for (int i = 0; i < 16; ++i) {
          if (i == 16 - PD && bar) rbar();
          o[i & 7] = mfma(fr[i & 7], pf[i >> 3], o[i & 7]);
          if (i + PD < 16) rdV(bc, i + PD); else if (!bar) {} else if (nextK) rdK(bn, i + PD - 16); else rdV(bn, i + PD - 16);
          if ((i & 7) == 3) ls = mfma(ones, pf[i >> 3], ls);
          if (i & 1) { if (bar) mx = fmaxf(fmaxf(sn[i >> 2][(i & 3) - 1], sn[i >> 2][i & 3]), i == 1 ? -INFINITY : mx); SB(); }
        }
      };
      auto check = [&]() INL {
        if (__any(!(mx * SL2E - m <= THR))) {
          const float mn = fmaxf(m, xmax4(mx) * SL2E);
          const float alpha = __builtin_amdgcn_exp2f(m - mn);
          m = mn; ls *= alpha;
#pragma unroll
          for (int i = 0; i < 8; ++i) o[i] *= alpha;
        }
        SB();
      };
      auto rdQ = [&](int bt) INL {
        const unsigned aq = AL() + bt * 32768 + cw * 4096;
#pragma unroll
        for (int kk = 0; kk < 4; ++kk) qf[kk] = ld128(aq + kk * 1024);
      };
      auto finish = [&](int bt) INL {
        const float inv = __builtin_amdgcn_rcpf(ls[0]);
        const unsigned aF = AF() + ((bt * 2 + (cw & 1)) * 16 + (cw >> 1) * 4) * 1024;
#pragma unroll
        for (int db = 0; db < 8; ++db) { st64(aF + db * 512, cvt4(o[db] * inv)); o[db] = Z4; }
        ls = Z4;
      };
#pragma unroll
      for (int i = 0; i < 8; ++i) o[i] = Z4;
      cbar();
      rdQ(0);
#pragma unroll
      for (int i = 0; i < PD; ++i) rdK(aK + sl, i);
      phaseA(sa, sb, true, false, true);
      m = sa[0][0];
#pragma unroll
      for (int i = 0; i < 4; ++i) m = fmaxf(fmaxf(fmaxf(sa[i][0], sa[i][1]), m), fmaxf(sa[i][2], sa[i][3]));
      m = xmax4(m) * SL2E;
#pragma unroll 1
      for (int j = 0; j < 14; j += 2) {
        phaseA(sb, sa, true, true, false);
        if (j == 6) rdQ(1);
        phaseB(sb, true, true); check();
        phaseA(sa, sb, true, true, false); phaseB(sa, true, true);
        if (j == 6) { finish(0); m = xmax4(mx) * SL2E; STAMP(2); } else check();
      }
      phaseA(sb, sa, true, true, false); phaseB(sb, true, false); check();
      phaseA(sa, sb, false, true, false); phaseB(sa, false, false);
      finish(1);
      STAMP(3);
    }
    {
      const unsigned l = fresh(lane), ao = L0 + RING + ((l >> 4) & 1) * 16384 + (l >> 5) * 256 + (l & 15) * 16;
      gemm([&](unsigned ks) INL { return ao + ks * 1024; }, 512, 32768, false);
    }
    STAMP(4);
#pragma unroll
    for (int i = 0; i < 8; ++i) {
      const f32x16& v = acc[i >> 2][(i >> 1) & 1];
      const int r0 = (i & 1) * 8;
      __builtin_amdgcn_raw_buffer_store_b128(cvt8(f32x4{v[r0], v[r0 + 1], v[r0 + 2], v[r0 + 3]}, f32x4{v[r0 + 4], v[r0 + 5], v[r0 + 6], v[r0 + 7]}), rt, lane * 16,
                                             WS_TILES_A + ((2 * idx + ((i >> 1) & 1)) * 8 + xcd) * TILE_B + (((i >> 2) * 8 + cw) * 2 + (i & 1)) * 1024, 16);
    }
    asm volatile("s_waitcnt vmcnt(0)" ::: "memory");
    if (lane < 2) __hip_atomic_store((GAS u64*)(a.ws + WS_FLAG + (((2 * idx + lane) * 8 + cw) * 32 + 16 + xcd) * 8), tag, RLX);
  } else {
    unsigned aq, ax0;
    {
      const unsigned l = fresh(lane), g = l >> 4, qq = (l >> 2) & 3;
      aq = L0 + (8 * g + qq) * 256 + (((unsigned)cw ^ (qq ^ (4 * (g & 1)))) << 5) + 8 * (l & 3);
      ax0 = AK();
    }
    const unsigned al = AL() + poff, aF = AF() + cw * 512 + poff;
    f32x4 qa[2];
    u32x4 wf[2][2], xf[2][4];
    auto qrd = [&](int st, int p) INL {
      const unsigned ii = (st + bB) & 7, b = aq + sl, ax = (ax0 + (RING + 0u + (ii >> 1) * 8192)) ^ ((ii & 1) << 7);
#pragma unroll
      for (int ks = 0; ks < 2; ++ks) wf[p][ks] = ldtr(b + ks * 8192, (b ^ 128) + 1024 + ks * 8192);
#pragma unroll
      for (int i = 0; i < 4; ++i) xf[p][i] = ld128((ax ^ ((i >> 1) << 6)) + (i & 1) * 4096);
      sl = nxt(sl);
      SB();
    };
    auto qmm = [&](int p) INL {
#pragma unroll
      for (int i = 0; i < 4; ++i) qa[i & 1] = mfma(wf[p][i >> 1], xf[p][i], qa[i & 1]);
      SB();
    };
    auto qrest = [&]() INL {
#pragma unroll 1
      for (int st = 1; st < 7; st += 2) {
        rbar(); qrd(st, 1); qmm(0);
        rbar(); qrd(st + 1, 0); qmm(1);
      }
      rbar(); qrd(7, 1); qmm(0);
    };
    auto qwr = [&]() INL {
#pragma unroll
      for (int qb = 0; qb < 2; ++qb) st64(aF + Q_OFF + qb * 4096, cvt4(qa[qb]));
      SB();
    };
    qa[0] = Z4; qa[1] = Z4;
    rbar(); qrd(0, 0); qrest(); qmm(1); qwr();
    STAMP(8);
    cbar();
    f32x4 oa[4][2];
#pragma unroll
    for (int nb = 0; nb < 4; ++nb) { oa[nb][0] = Z4; oa[nb][1] = Z4; }
#pragma unroll 1
    for (int u = 0; u < nu; ++u) {
      f32x4 s[8];
      float mx = -INFINITY;
      const unsigned aS = L0 + RING + poff + ST_OFF + (fresh(lane) & 15) * 16;
      const unsigned aP = al + ((cw >> 2) * 2048 + (cw & 1) * 1024 + ((cw >> 1) & 1) * 8);
      const unsigned aV = AV() ^ ((unsigned)cw << 5), aw = AW();
      {
        u32x4 qf[4], kf[2][4];
        const unsigned aK = ax0 + (cw >> 1) * 4096;
        auto kmm = [&](int i) INL {
          s[i] = Z4;
#pragma unroll
          for (int kk = 0; kk < 4; ++kk) s[i] = mfma(kf[i & 1][kk], qf[kk], s[i]);
          if (i > 0) mx = fmaxf(fmaxf(fmaxf(s[i - 1][0], s[i - 1][1]), mx), fmaxf(s[i - 1][2], s[i - 1][3]));
          SB();
        };
#pragma unroll
        for (int i = 0; i < 8; ++i) {
          if (i > 0 || u > 0) rbar();
          const unsigned b = aK + sl;
#pragma unroll
          for (int kk = 0; kk < 4; ++kk) kf[i & 1][kk] = ld128(b ^ (kk << 6));
          if (i == 0) {
#pragma unroll
            for (int kk = 0; kk < 4; ++kk) qf[kk] = ld128(al + (Q_OFF + (cw & 1) * 4096) + kk * 1024);
          }
          sl = nxt(sl);
          SB();
          if (i > 0 && i != 6) kmm(i - 1);
          if (i == 5) {
            kmm(5);
            if (early && u > 0) {
#pragma unroll
              for (int t = 0; t <= 8; ++t) {
                if (t < 8) {
                  rbar();
                  const unsigned bw = aw + sl;
#pragma unroll
                  for (int nb = 0; nb < 2; ++nb) wf[t & 1][nb] = ldtr(bw ^ (nb << 5), (bw ^ (nb << 5) ^ 128) + 2048);
                  if (!(t & 1)) {
#pragma unroll
                    for (int qb = 0; qb < 2; ++qb) xf[(t >> 1) & 1][qb] = ld128(al + (((t >> 1) + bB) & 3) * 1024 + qb * 4096);
                  }
                  sl = nxt(sl);
                  SB();
                }
                if (t > 0) {
                  const int p = t - 1;
#pragma unroll
                  for (int j = 0; j < 4; ++j) oa[(p & 1) * 2 + (j >> 1)][j & 1] = mfma(wf[p & 1][j >> 1], xf[(p >> 1) & 1][j & 1], oa[(p & 1) * 2 + (j >> 1)][j & 1]);
                  SB();
                }
              }
            }
            if (early && u + 1 < nu) {
              qa[0] = Z4; qa[1] = Z4;
              rbar(); qrd(0, 0); qrest(); qmm(1); qwr();
            }
          }
        }
        kmm(7);
      }
      if (u == 0) STAMP(1);
      mx = xmax4(fmaxf(fmaxf(fmaxf(s[7][0], s[7][1]), mx), fmaxf(s[7][2], s[7][3])));
      if (lane < 16) *(LAS float*)(uintptr_t)(aS + (cw & 1) * 256 + (cw >> 1) * 4) = mx;
      cbar();
      const f32x4 m0 = ldf4(aS + (cw & 1) * 256);
      const float mn = SL2E * fmaxf(fmaxf(m0[0], m0[1]), fmaxf(m0[2], m0[3]));
      f32x4 ps = Z4;
      auto pexp = [&](int i) INL {
        const f32x4 e = s[i] * SL2E - mn;
        f32x4 p;
#pragma unroll
        for (int r = 0; r < 4; ++r) p[r] = __builtin_amdgcn_exp2f(e[r]);
        ps += p;
        st64(aP + (i & 3) * 4096, cvt4(p));
        SB();
      };
      pexp(0); pexp(1);
      f32x4 o[2] = {Z4, Z4};
      u32x4 vf[2][2], pf[2][4];
      auto vrd = [&](int i) INL {
        const unsigned b = aV + sl;
#pragma unroll
        for (int ks = 0; ks < 2; ++ks) vf[i & 1][ks] = ldtr(b + ks * 8192, b + ks * 8192 + 4096);
#pragma unroll
        for (int j = 0; j < 4; ++j) pf[i & 1][j] = ld128(al + ((i & 3) * 4096 + j * 1024));
        sl = nxt(sl);
        SB();
      };
      auto vmm = [&](int i) INL {
#pragma unroll
        for (int j = 0; j < 4; ++j) o[j & 1] = mfma(vf[i & 1][j >> 1], pf[i & 1][j], o[j & 1]);
        SB();
      };
#pragma unroll
      for (int i = 0; i < 6; ++i) {
        if (i == 0) cbar(); else { SB(); asm volatile("s_waitcnt lgkmcnt(9)\n\ts_barrier" ::: "memory"); SB(); }
        vrd(i);
        if (i > 0) vmm(i - 1);
        pexp(i + 2);
      }
      {
        const float pt = xsum4((ps[0] + ps[1]) + (ps[2] + ps[3]));
        if (lane < 16) *(LAS float*)(uintptr_t)(aS + 512 + (cw & 1) * 256 + (cw >> 1) * 4) = pt;
        SB();
      }
      auto vtail = [&]() INL {
        cbar(); vrd(6);
        float inv[2];
#pragma unroll
        for (int qb = 0; qb < 2; ++qb) { const f32x4 l0 = ldf4(aS + 512 + qb * 256); inv[qb] = __builtin_amdgcn_rcpf((l0[0] + l0[1]) + (l0[2] + l0[3])); }
        vmm(6);
        rbar(); vrd(7); vmm(7);
#pragma unroll
        for (int qb = 0; qb < 2; ++qb) st64(aF + qb * 4096, cvt4(o[qb] * inv[qb]));
        cbar();
      };
      int lim = early ? nu - 1 : 0;
      if (u < lim) { vmm(5); vtail(); }
      asm volatile("" : "+s"(lim));
      if (u >= lim) {
        u32x4 wo[4][4];
        auto wrd = [&](int i) INL {
          const unsigned b = aw + sl;
#pragma unroll
          for (int nb = 0; nb < 2; ++nb) wo[i >> 1][(i & 1) * 2 + nb] = ldtr(b ^ (nb << 5), (b ^ (nb << 5) ^ 128) + 2048);
          sl = nxt(sl);
          SB();
        };
        if (u + 1 < nu) {
          qa[0] = Z4; qa[1] = Z4;
          rbar(); qrd(0, 0); vmm(5);
          qrest();
          rbar(); wrd(0); qmm(1);
          rbar(); wrd(1); qwr();
        } else {
          rbar(); wrd(0); vmm(5);
          rbar(); wrd(1);
        }
#pragma unroll
        for (int i = 2; i < 8; ++i) {
          SB(); asm volatile("s_waitcnt lgkmcnt(4)\n\ts_barrier" ::: "memory"); SB();
          wrd(i);
        }
        vtail();
        u32x4 of[2][4];
#pragma unroll
        for (int qb = 0; qb < 2; ++qb)
#pragma unroll
          for (int kk = 0; kk < 4; ++kk) of[qb][kk] = ld128(al + ((kk + bB) & 3) * 1024 + qb * 4096);
        SB();
#pragma unroll
        for (int qb = 0; qb < 2; ++qb)
#pragma unroll
          for (int kk = 0; kk < 4; ++kk)
#pragma unroll
            for (int nb = 0; nb < 4; ++nb) oa[nb][qb] = mfma(wo[kk][nb], of[qb][kk], oa[nb][qb]);
      }
      STAMP(2 + u);
    }
#pragma unroll
    for (int i = 0; i < 4; ++i)
      __builtin_amdgcn_raw_buffer_store_b128(cvt8(oa[(i & 1) * 2][i >> 1], oa[(i & 1) * 2 + 1][i >> 1]), rt, lane * 16, (bB * 12 + slotB) * TILE_B + (((i & 1) * 8 + cw) * 2 + (i >> 1)) * 1024, 16);
    asm volatile("s_waitcnt vmcnt(0)" ::: "memory");
    if (lane == 0) __hip_atomic_store((GAS u64*)(a.ws + WS_FLAG + ((bB * 8 + cw) * 32 + slotB) * 8), tag, RLX);
  }
  STAMP(5);

  {
    const int rb = blockIdx.x >> 4, pj = blockIdx.x & 15, pc = pj * 2 + ((cw >> 1) & 1), hb = cw & 1;
    const bool wB = cw < 4;
    const GAS u64* fp = (const GAS u64*)(a.ws + WS_FLAG + ((rb * 8 + (pj & 7)) * 32 + (wB ? (lane < 12 ? lane : 0) : 16 + (lane & 7))) * 8);
    for (int spin = 0; spin < NSPIN; ++spin) {
      if (__all(__hip_atomic_load(fp, RLX) == tag)) break;
      __builtin_amdgcn_s_sleep(SLP);
    }
    __builtin_amdgcn_fence(__ATOMIC_ACQUIRE, "wavefront");
    STAMP(6);
    const unsigned so = (wB ? rb * 12 * TILE_B : WS_TILES_A + rb * 8 * TILE_B) + pc * 1024 + hb * 512;
    const unsigned vo = (lane >> 5) * TILE_B + (lane & 31) * 16;
    u32x4 t[6];
#pragma unroll
    for (int i = 0; i < 6; ++i) t[i] = __builtin_amdgcn_raw_buffer_load_b128(rt, (i < 4 || wB) ? vo : 0x7fffff00u, so + i * 2 * TILE_B, 16);
    f32x4 s0 = Z4, s1 = Z4;
#pragma unroll
    for (int i = 0; i < 6; ++i) { s0 += bf4(t[i][0], t[i][1]); s1 += bf4(t[i][2], t[i][3]); }
    f32x4 r;
#pragma unroll
    for (int j = 0; j < 4; ++j) {
      const auto x = __builtin_amdgcn_permlane32_swap(__float_as_uint(s0[j]), __float_as_uint(s1[j]), false, false);
      r[j] = __uint_as_float(x[0]) + __uint_as_float(x[1]);
    }
    const unsigned sl_ = hb * 32 + (lane & 31);
    const unsigned row = wB ? (pc & 1) * 16 + (sl_ & 15) : (lane & 31);
    const unsigned col = wB ? 512 + 16 * (lane >> 5) + 4 * (sl_ >> 4) : 16 * (pc & 1) + 8 * (lane >> 5) + 4 * hb;
    *(GAS u32x2*)(a.out + ((size_t)(rb * SQ + row) * 1024 + 32 * (pc >> 1) + col) * 2) = cvt4(r);
  }
  STAMP(7);
}

extern "C" void kernel_launch(void* const* d_in, const int* in_sizes, int n_in, void* d_out, int out_size, void* d_ws, size_t ws_size,
                              hipStream_t stream) {
  Args a;
  for (int br = 0; br < 2; ++br) {
    a.x[br] = d_in[5 * br + 0];
    a.k[br] = d_in[5 * br + 1];
    a.v[br] = d_in[5 * br + 2];
    a.wq[br] = d_in[5 * br + 3];
    a.wo[br] = d_in[5 * br + 4];
  }
  a.ws = (char*)d_ws;
  a.out = (char*)d_out;
  (void)hipFuncSetAttribute((const void*)fused_k, hipFuncAttributeMaxDynamicSharedMemorySize, LDS_BYTES);
  fused_k<<<256, 768, LDS_BYTES, stream>>>(a);
}
```

```cpp
#include <hip/hip_runtime.h>
#include <stdint.h>

#define LAS __attribute__((address_space(3)))
#define GAS __attribute__((address_space(1)))
typedef unsigned u32x4 __attribute__((ext_vector_type(4)));
typedef unsigned u32x2 __attribute__((ext_vector_type(2)));
typedef float f32x4 __attribute__((ext_vector_type(4)));
typedef float f32x2 __attribute__((ext_vector_type(2)));
typedef short bf16x8 __attribute__((ext_vector_type(8)));
typedef short s16x4 __attribute__((ext_vector_type(4)));
typedef __bf16 bf16x2 __attribute__((ext_vector_type(2)));
typedef __amdgpu_buffer_rsrc_t rsrc_t;
typedef unsigned long long u64;

constexpr int NB = 16, SQ = 32, DM = 512, NH = 32, SK = 512, DH = 128;
constexpr float SL2E = 0.088388347648318447f * 1.4426950408889634f;
constexpr unsigned HT = 16384, RING = 6 * HT;
constexpr unsigned Q_OFF = 16384, ST_OFF = Q_OFF + 8192;
constexpr unsigned LDS_BYTES = 160 * 1024;
constexpr int NLD = 4;
constexpr unsigned KVB = SK * DH * 2;
constexpr unsigned WS_FLAG = 4096, WS_TILES = 1u << 20, TILE_B = 32768;
constexpr unsigned WS_TILES_A = 192 * TILE_B, WS_TILES_BYTES = 320 * TILE_B;

struct Args {
  const void* x[2]; const void* k[2]; const void* v[2]; const void* wq[2]; const void* wo[2];
  char* ws; char* out;
};

#define SB() __builtin_amdgcn_sched_barrier(0)
#define INL __attribute__((always_inline))
#define TAGC 0x9E3779B97F4A7C15ull
#define RLX __ATOMIC_RELAXED, __HIP_MEMORY_SCOPE_AGENT
#define Z4 f32x4{0.f, 0.f, 0.f, 0.f}
__device__ __forceinline__ rsrc_t mkrsrc(const void* p, unsigned bytes) { return __builtin_amdgcn_make_buffer_rsrc((void*)p, 0, bytes, 0x00020000); }
template <int AUX, int N> __device__ __forceinline__ void dma(rsrc_t r, unsigned lds, const unsigned (&v)[4], unsigned soff) {
  __builtin_amdgcn_raw_ptr_buffer_load_lds(r, (LAS void*)(uintptr_t)lds, 16, v[0], soff, 0, AUX);
  __builtin_amdgcn_raw_ptr_buffer_load_lds(r, (LAS void*)(uintptr_t)lds, 16, v[1], soff, 1024, AUX);
  if (N > 2) {
    __builtin_amdgcn_raw_ptr_buffer_load_lds(r, (LAS void*)(uintptr_t)lds, 16, v[2], soff, 2048, AUX);
    __builtin_amdgcn_raw_ptr_buffer_load_lds(r, (LAS void*)(uintptr_t)lds, 16, v[3], soff, 3072, AUX);
  }
}
template <int N> __device__ __forceinline__ void waitb() { asm volatile("s_waitcnt vmcnt(%0)\n\ts_barrier" :: "i"(N) : "memory"); }
#define WAITB(N) waitb<N>()
__device__ __forceinline__ void lbar() { asm volatile("s_barrier" ::: "memory"); }
__device__ __forceinline__ void rbar() { SB(); asm volatile("s_barrier" ::: "memory"); SB(); }
__device__ __forceinline__ void cbar() { SB(); asm volatile("s_waitcnt lgkmcnt(0)\n\ts_barrier" ::: "memory"); SB(); }
__device__ __forceinline__ f32x4 mfma(u32x4 a, u32x4 b, f32x4 c) {
  return __builtin_amdgcn_mfma_f32_16x16x32_bf16(__builtin_bit_cast(bf16x8, a), __builtin_bit_cast(bf16x8, b), c, 0, 0, 0);
}
typedef float f32x16 __attribute__((ext_vector_type(16)));
__device__ __forceinline__ f32x16 mfma32(u32x4 a, u32x4 b, f32x16 c) {
  return __builtin_amdgcn_mfma_f32_32x32x16_bf16(__builtin_bit_cast(bf16x8, a), __builtin_bit_cast(bf16x8, b), c, 0, 0, 0);
}
__device__ __forceinline__ unsigned cvtpk(float lo, float hi) { return __builtin_bit_cast(unsigned, __builtin_convertvector(f32x2{lo, hi}, bf16x2)); }
__device__ __forceinline__ u32x2 cvt4(f32x4 v) { return u32x2{cvtpk(v[0], v[1]), cvtpk(v[2], v[3])}; }
__device__ __forceinline__ u32x4 cvt8(f32x4 a, f32x4 b) { return u32x4{cvtpk(a[0], a[1]), cvtpk(a[2], a[3]), cvtpk(b[0], b[1]), cvtpk(b[2], b[3])}; }
__device__ __forceinline__ f32x4 bf4(unsigned lo, unsigned hi) { return f32x4{__uint_as_float(lo << 16), __uint_as_float(lo & 0xffff0000u), __uint_as_float(hi << 16), __uint_as_float(hi & 0xffff0000u)}; }
__device__ __forceinline__ u32x4 ld128(unsigned a) { return *(const LAS u32x4*)(uintptr_t)a; }
__device__ __forceinline__ f32x4 ldf4(unsigned a) { return *(const LAS f32x4*)(uintptr_t)a; }
__device__ __forceinline__ void st64(unsigned a, u32x2 v) { *(LAS u32x2*)(uintptr_t)a = v; }
__device__ __forceinline__ u32x4 ldtr(unsigned a0, unsigned a1) {
  const u32x2 lo = __builtin_bit_cast(u32x2, __builtin_amdgcn_ds_read_tr16_b64_v4i16((LAS s16x4*)(uintptr_t)a0));
  const u32x2 hi = __builtin_bit_cast(u32x2, __builtin_amdgcn_ds_read_tr16_b64_v4i16((LAS s16x4*)(uintptr_t)a1));
  return u32x4{lo[0], lo[1], hi[0], hi[1]};
}
__device__ __forceinline__ float xmax4(float x) {
  auto s = __builtin_amdgcn_permlane16_swap(__float_as_uint(x), __float_as_uint(x), false, false);
  x = fmaxf(__uint_as_float(s[0]), __uint_as_float(s[1]));
  auto t = __builtin_amdgcn_permlane32_swap(__float_as_uint(x), __float_as_uint(x), false, false);
  return fmaxf(__uint_as_float(t[0]), __uint_as_float(t[1]));
}
__device__ __forceinline__ float xsum4(float x) {
  auto s = __builtin_amdgcn_permlane16_swap(__float_as_uint(x), __float_as_uint(x), false, false);
  x = __uint_as_float(s[0]) + __uint_as_float(s[1]);
  auto t = __builtin_amdgcn_permlane32_swap(__float_as_uint(x), __float_as_uint(x), false, false);
  return __uint_as_float(t[0]) + __uint_as_float(t[1]);
}
__device__ __forceinline__ unsigned nxt(unsigned s) { s += HT; return s == RING ? 0u : s; }
__device__ __forceinline__ unsigned fresh(unsigned x) { asm volatile("" : "+v"(x)); return x; }
#define THR 8.f
#define PACES 14
#define PAT ((idx % 3) == 0 ? 15 : (idx % 3) == 1 ? 3 : 12)
#define NSPIN (1 << 16)

__global__ void __launch_bounds__(768, 3) fused_k(Args a) {
  extern __shared__ __attribute__((aligned(16))) char smem[];
  const unsigned lane = threadIdx.x & 63;
  const int w = __builtin_amdgcn_readfirstlane(threadIdx.x >> 6);
  const int xcd = blockIdx.x & 7, pi = blockIdx.x >> 3;
  const int idx = (pi & 1) ? 8 + (pi >> 1) : (pi < 16 ? pi >> 1 : 16 + (pi >> 1));
  const bool isA = idx < 8;
  const int nu = idx < 24 ? 3 : 2;
  const int bB = nu == 3 ? idx - 8 : (xcd & 1) * 8 + (idx - 24);
  const int slotB = nu == 3 ? xcd : 8 + (xcd >> 1);
  auto headB = [&](int u) INL { int x = u + bB % 3; x = x >= 3 ? x - 3 : x; return nu == 3 ? 4 * xcd + x : 4 * (xcd ^ ((u ^ bB) & 1)) + 3; };
  const int pat = PAT, eqm = pat & 3, dfm = (pat >> 2) & (nu == 3 ? 3 : 1);
  constexpr unsigned poff = 32768;
  const unsigned L0 = (unsigned)(uintptr_t)(LAS char*)smem;

  if (__builtin_expect(w < NLD, 1)) {
    const unsigned l = w;
    const rsrc_t rx = mkrsrc(isA ? a.x[0] : a.x[1], NB * SQ * DM * 2), rq = mkrsrc(isA ? a.wq[0] : a.wq[1], DM * NH * DH * 2);
    const unsigned s16 = lane & 15, e4 = lane >> 4, e2 = lane >> 5;
    auto offK = [&](int q, unsigned rs) INL { return (16 * l + e4) * rs + q * (4 * rs - 1024) + ((s16 ^ (4 * q + e4)) << 4); };
    auto offT = [&](int q, unsigned rs) INL { return (16 * l + e4) * rs + q * (4 * rs - 1024) + ((s16 ^ ((e4 ^ (((q ^ (q >> 1)) & 1) * 4)) << 1)) << 4); };
    auto offW = [&](int q, unsigned rs) INL { return (8 * l + e2) * rs + q * (2 * rs - 1024) + (((lane & 31) * 16) ^ (((2 * q + e2) ^ (4 * (l & 1))) * 32)); };
    const unsigned pl = L0 + l * 4096;
    unsigned sl = 0;
    auto wt = [&](rsrc_t r, const unsigned (&v)[4], unsigned so) INL { dma<0, 4>(r, pl + sl, v, so); sl = nxt(sl); };
    auto kvt = [&](rsrc_t r, const unsigned (&v)[4], unsigned so) INL {
      if (PACES && nu == 2) __builtin_amdgcn_s_sleep(PACES);
      dma<2, 4>(r, pl + sl, v, so); sl = nxt(sl);
    };
    unsigned vX[4], vQ[4], vK[4], vV[4], vO[4];
    auto late = [&]() INL {
      __builtin_amdgcn_s_setprio(3);
      SB();
#pragma unroll
      for (int q = 0; q < 4; ++q) { vK[q] = fresh(offK(q, 256)); vV[q] = fresh(offT(q, 256)); vO[q] = fresh(offW(q, 1024)); }
      asm volatile("" :: "s"(a.k[0]), "s"(a.k[1]), "s"(a.v[0]), "s"(a.v[1]), "s"(a.wo[0]), "s"(a.wo[1]));
      SB();
    };
    if (__builtin_expect(!isA, 1)) {
#pragma unroll
      for (int q = 0; q < 4; ++q) { vX[q] = (8 * l + e4) * 1024 + q * 3072 + ((s16 ^ (8 * (l & 1) + 4 * q + e4)) << 4); vQ[q] = offT(q, 8192); }
      auto xt = [&](int i) INL { const unsigned c4 = ((bB >> 1) + i) & 3; dma<0, 2>(rx, L0 + RING + c4 * 8192 + l * 2048, vX, (unsigned)bB * (SQ * DM * 2) + c4 * 256); };
      auto qt = [&](int h, int i) INL { wt(rq, vQ, ((i + bB) & 7) * (64 * 8192) + h * 256); };
      int h = headB(0);
      xt(0); qt(h, 0); xt(1); qt(h, 1); xt(2); qt(h, 2); xt(3); qt(h, 3);
      late();
      WAITB(18); qt(h, 4);
      WAITB(16); qt(h, 5);
      WAITB(14); qt(h, 6);
      WAITB(12); qt(h, 7);
      {
        const rsrc_t rk = mkrsrc(a.k[1], NB * NH * KVB), rv = mkrsrc(a.v[1], NB * NH * KVB), ro = mkrsrc(a.wo[1], DM * NH * DH * 2);
        unsigned kvo = (unsigned)(bB * NH + h) * KVB;
        int hp = h;
        auto ot = [&](int hh, int i) INL { wt(ro, vO, hh * (128 * 1024) + (((i >> 1) + bB) & 3) * (32 * 1024) + (i & 1) * 512); };
#pragma unroll
        for (int i = 0; i < 6; ++i) { WAITB(12); kvt(rk, vK, kvo + i * HT); }
#pragma unroll 1
        for (int u = 0;; ++u) {
          const int hn = headB(u + 1);
          if (((dfm << 1) >> u) & 1) {
#pragma unroll
            for (int i = 0; i < 8; ++i) { WAITB(12); ot(hp, i); }
          }
          if (((eqm >> u) & 1) && u + 1 < nu) {
#pragma unroll
            for (int i = 0; i < 8; ++i) { WAITB(12); qt(hn, i); }
          }
          WAITB(12); kvt(rk, vK, kvo + 6 * HT);
          WAITB(12); kvt(rk, vK, kvo + 7 * HT);
#pragma unroll
          for (int i = 0; i < 3; ++i) { WAITB(12); kvt(rv, vV, kvo + i * HT); }
          WAITB(12); lbar();
#pragma unroll
          for (int i = 3; i < 6; ++i) kvt(rv, vV, kvo + i * HT);
          WAITB(20); WAITB(16);
          if (!((eqm >> u) & 1) && u + 1 < nu) {
#pragma unroll
            for (int i = 0; i < 8; ++i) { WAITB(12); qt(hn, i); }
          }
          if (!((dfm >> u) & 1)) {
#pragma unroll
            for (int i = 0; i < 8; ++i) { WAITB(12); ot(h, i); }
          }
          WAITB(12); kvt(rv, vV, kvo + 6 * HT);
          WAITB(12); kvt(rv, vV, kvo + 7 * HT);
          if (u + 1 == nu) break;
          hp = h; h = hn;
          kvo = (unsigned)(bB * NH + h) * KVB;
#pragma unroll
          for (int i = 0; i < 3; ++i) { WAITB(12); kvt(rk, vK, kvo + i * HT); }
          WAITB(12); lbar();
#pragma unroll
          for (int i = 3; i < 6; ++i) kvt(rk, vK, kvo + i * HT);
          WAITB(20); WAITB(16);
        }
      }
    } else {
#pragma unroll
      for (int q = 0; q < 4; ++q) { vX[q] = offK(q, 1024); vQ[q] = offW(q, 8192); }
      auto xt = [&](int i) INL { const unsigned c4 = ((idx >> 1) + i) & 3; dma<0, 4>(rx, pl + RING + c4 * HT, vX, (unsigned)idx * (2 * SQ * DM * 2) + c4 * 256); };
      auto qt = [&](int i) INL { wt(rq, vQ, ((2 * idx + (i >> 1)) & 15) * (32 * 8192) + xcd * 1024 + (i & 1) * 512); };
      xt(0); qt(0); qt(1); qt(2); xt(1); qt(3);
      late();
      const rsrc_t rk = mkrsrc(a.k[0], NB * 8 * KVB), rv = mkrsrc(a.v[0], NB * 8 * KVB), ro = mkrsrc(a.wo[0], DM * NH * DH * 2);
      WAITB(12); qt(4); xt(2); qt(5); xt(3);
      WAITB(16); qt(6); qt(7);
#pragma unroll 2
      for (int i = 8; i < 32; i += 2) { WAITB(8); qt(i); qt(i + 1); }
      auto kvo = [&](int j) INL { return (unsigned)((2 * idx + (j >> 3)) * 8 + xcd) * KVB + (j & 7) * HT; };
      auto ot = [&](int i) INL { wt(ro, vO, xcd * (512 * 1024) + ((2 * idx + (i >> 1)) & 15) * (32 * 1024) + (i & 1) * 512); };
      WAITB(8); kvt(rk, vK, kvo(0)); kvt(rk, vK, kvo(1));
      WAITB(8); kvt(rv, vV, kvo(0)); kvt(rk, vK, kvo(2));
      lbar();
      WAITB(12); kvt(rv, vV, kvo(1));
#pragma unroll 1
      for (int j = 2; j < 15; ++j) {
        WAITB(12); kvt(rk, vK, kvo(j + 1));
        WAITB(12); kvt(rv, vV, kvo(j));
      }
      WAITB(12); kvt(rv, vV, kvo(15));
#pragma unroll
      for (int i = 0; i < 4; ++i) { WAITB(12); ot(i); }
#pragma unroll 2
      for (int i = 4; i < 32; i += 2) { WAITB(8); ot(i); ot(i + 1); }
      WAITB(8); WAITB(0);
      return;
    }
    WAITB(12); WAITB(8); WAITB(4); WAITB(0);
    if (blockIdx.x == 240 && w == 0) {
      lbar(); lbar();
      const u64 tag = __hip_atomic_load((const GAS u64*)a.ws, RLX) + TAGC;
      const unsigned sidx = lane & 31, su = (sidx < 12 || (sidx >= 16 && sidx < 24)) ? sidx : 0;
      for (int spin = 0; spin < NSPIN; ++spin) {
        bool ok = true;
#pragma unroll
        for (int i = 0; i < 8; ++i) ok &= __hip_atomic_load((const GAS u64*)(a.ws + WS_FLAG + ((i * 2 + (lane >> 5)) * 256 + su) * 8), RLX) == tag;
        if (__all(ok)) break;
        __builtin_amdgcn_s_sleep(4);
      }
      if (lane == 0) __hip_atomic_store((GAS u64*)a.ws, tag, RLX);
    }
    return;
  }

  const int cw = w - NLD;
  if (nu == 2) {
    const GAS char* p = (const GAS char*)a.wq[0] + (idx - 24) * (64 * 8192) + xcd * 1024 + (size_t)(cw * 8 + (lane >> 4)) * 8192 + (lane & 15) * 64;
    const unsigned t0 = *(const GAS unsigned*)p, t1 = *(const GAS unsigned*)(p + 4 * 8192);
    asm volatile("" :: "v"(t0), "v"(t1));
  }
  const u64 ep = __hip_atomic_load((const GAS u64*)a.ws, RLX);
  u64 tag;
  auto mktag = [&]() INL { SB(); tag = (((u64)(unsigned)__builtin_amdgcn_readfirstlane((unsigned)(ep >> 32)) << 32) | (unsigned)__builtin_amdgcn_readfirstlane((unsigned)ep)) + TAGC; SB(); };
  unsigned sl = 0;
  auto AK = [&]() INL { const unsigned l = fresh(lane), c = l & 15, g = l >> 4; return L0 + c * 256 + ((g ^ c) << 4); };
  auto AV = [&]() INL { const unsigned l = fresh(lane), g = l >> 4, qq = (l >> 2) & 3;
                    return L0 + (4 * g + qq) * 256 + (((4 * (g & 1) + qq) ^ (4 * (g >> 1))) << 5) + 8 * (l & 3); };
  auto AW = [&]() INL { const unsigned l = fresh(lane), g = l >> 4, qq = (l >> 2) & 3;
                    return L0 + (8 * g + qq) * 512 + ((((unsigned)cw * 2) ^ (qq ^ (4 * (g & 1)))) << 5) + 8 * (l & 3); };
  auto AF = [&]() INL { const unsigned l = fresh(lane); return L0 + RING + ((l & 15) + 16 * (l >> 5)) * 16 + 8 * ((l >> 4) & 1); };
  auto AL = [&]() INL { return L0 + RING + fresh(lane) * 16; };

  const rsrc_t rt = mkrsrc(a.ws + WS_TILES, WS_TILES_BYTES);
  if (isA) {
    f32x16 acc[2][2];
    auto gemm = [&](auto xaddr, unsigned xh, unsigned xs, bool endbar) INL {
      u32x4 xf[2][4], wf[2][2];
      const unsigned l = fresh(lane), h = l >> 5, qq = (l >> 2) & 3;
      const unsigned aW = L0 + (8 * h + qq) * 512 + ((((unsigned)cw * 2 + ((l >> 4) & 1)) ^ (qq ^ (4 * h))) << 5) + 8 * (l & 3);
      auto step = [&](int ks, int hf, int xp, int domm, bool dord) INL {
        const unsigned b = aW + sl, ax = xaddr((2 * idx + ks) & 15);
        const int ph = hf ^ 1, px = hf ? xp : xp ^ 1;
#pragma unroll
        for (int i = 0; i < 4; ++i) {
          if (domm) acc[ph][i & 1] = mfma32(wf[ph][i >> 1], xf[px][i], (domm == 2 && i < 2) ? f32x16{} : acc[ph][i & 1]);
          SB();
          if (dord && i < 2) {
            wf[hf][i] = ldtr(b + i * 8192, (b ^ 128) + 2048 + i * 8192);
            if (hf == 0) { xf[xp][2 * i] = ld128(ax ^ (i * xh)); xf[xp][2 * i + 1] = ld128((ax ^ (i * xh)) + xs); }
            SB();
          }
        }
        if (dord) sl = nxt(sl);
      };
      cbar(); step(0, 0, 0, 0, true); step(0, 1, 0, 2, true);
      cbar(); step(1, 0, 1, 2, true); step(1, 1, 1, 1, true);
#pragma unroll 1
      for (int ks = 2; ks < 16; ks += 2) {
        cbar(); step(ks, 0, 0, 1, true); step(ks, 1, 0, 1, true);
        cbar(); step(ks + 1, 0, 1, 1, true); step(ks + 1, 1, 1, 1, true);
      }
      if (endbar) rbar();
      step(0, 0, 0, 1, false);
    };
    {
      const unsigned l = fresh(lane), ax = L0 + RING + (l & 31) * 256 + (((l >> 5) ^ (l & 15)) << 4);
      gemm([&](unsigned ks) INL { return (ax + (ks >> 2) * HT) ^ ((ks & 3) << 6); }, 32, 8192, true);
    }
    mktag();
    {
      const unsigned l = fresh(lane);
      const unsigned aF = L0 + RING + ((cw >> 2) * 8 + (cw & 3)) * 1024 + ((l >> 4) & 1) * 4096 + (l & 15) * 16 + (l >> 5) * 8;
#pragma unroll
      for (int i = 0; i < 4; ++i)
#pragma unroll
        for (int j = 0; j < 4; ++j) {
          const f32x16& v = acc[i >> 1][i & 1];
          st64(aF + (i & 1) * 32768 + (i >> 1) * 16384 + j * 256, cvt4(f32x4{v[4 * j], v[4 * j + 1], v[4 * j + 2], v[4 * j + 3]} * SL2E));
        }
    }
    {
      const unsigned aK = AK(), aV = AV();
      constexpr int PD = 7;
      u32x4 fr[8], qf[4], pf[2];
      f32x4 o[8], sa[4], sb[4], ls = Z4;
      auto rdK = [&](unsigned b, int i) INL { fr[i & 7] = ld128((b ^ ((i & 3) << 6)) + (i >> 2) * 4096); };
      auto rdV = [&](unsigned b, int i) INL { const unsigned ad = (b ^ ((i & 7) << 5)) + (i >> 3) * 8192; fr[i & 7] = ldtr(ad, ad + 4096); };
      f32x4 nm = Z4;
      float mx;
      const u32x4 ones = {0x3f803f80u, 0x3f803f80u, 0x3f803f80u, 0x3f803f80u};
      auto phaseA = [&](f32x4 (&sn)[4], f32x4 (&sc)[4], bool domm, bool dosm, bool nextK) INL {
        const unsigned bc = sl + aK;
        if (domm) sl = nxt(sl);
        const unsigned bn = sl + (nextK ? aK : aV);
#pragma unroll
        for (int i = 0; i < 16; ++i) {
          if (domm) {
            if (i == 16 - PD) rbar();
            sn[i >> 2] = mfma(fr[i & 7], qf[i & 3], (i & 3) ? sn[i >> 2] : nm);
            if (i + PD < 16) rdK(bc, i + PD); else if (nextK) rdK(bn, i + PD - 16); else rdV(bn, i + PD - 16);
          }
          if (dosm) {
            sc[i >> 2][i & 3] = __builtin_amdgcn_exp2f(sc[i >> 2][i & 3]);
            if (i & 1) pf[i >> 3][(i >> 1) & 3] = cvtpk(sc[i >> 2][(i & 3) - 1], sc[i >> 2][i & 3]);
          }
          if (i & 1) SB();
        }
      };
      auto phaseB = [&](f32x4 (&sn)[4], bool bar, bool nextK) INL {
        const unsigned bc = sl + aV;
        sl = nxt(sl);
        const unsigned bn = sl + (nextK ? aK : aV);
#pragma unroll
        for (int i = 0; i < 16; ++i) {
          if (i == 16 - PD && bar) rbar();
          o[i & 7] = mfma(fr[i & 7], pf[i >> 3], o[i & 7]);
          if (i + PD < 16) rdV(bc, i + PD); else if (!bar) {} else if (nextK) rdK(bn, i + PD - 16); else rdV(bn, i + PD - 16);
          if ((i & 7) == 3) ls = mfma(ones, pf[i >> 3], ls);
          if (i & 1) { if (bar) mx = fmaxf(fmaxf(sn[i >> 2][(i & 3) - 1], sn[i >> 2][i & 3]), i == 1 ? -INFINITY : mx); SB(); }
        }
      };
      auto shift = [&](f32x4 (&sn)[4], float d) INL {
        nm -= d;
#pragma unroll
        for (int i = 0; i < 4; ++i) sn[i] -= d;
      };
      auto check = [&](f32x4 (&sn)[4]) INL {
        if (__any(!(mx <= THR))) {
          const float d = fmaxf(xmax4(mx), 0.f), alpha = __builtin_amdgcn_exp2f(-d);
          shift(sn, d);
          ls *= alpha;
#pragma unroll
          for (int i = 0; i < 8; ++i) o[i] *= alpha;
        }
        SB();
      };
      auto rdQ = [&](int bt) INL {
        const unsigned aq = AL() + bt * 32768 + cw * 4096;
#pragma unroll
        for (int kk = 0; kk < 4; ++kk) qf[kk] = ld128(aq + kk * 1024);
      };
      auto finish = [&](int bt) INL {
        const float inv = __builtin_amdgcn_rcpf(ls[0]);
        const unsigned aF = AF() + ((bt * 2 + (cw & 1)) * 16 + (cw >> 1) * 4) * 1024;
#pragma unroll
        for (int db = 0; db < 8; ++db) { st64(aF + db * 512, cvt4(o[db] * inv)); o[db] = Z4; }
        ls = Z4;
      };
#pragma unroll
      for (int i = 0; i < 8; ++i) o[i] = Z4;
      cbar();
      rdQ(0);
#pragma unroll
      for (int i = 0; i < PD; ++i) rdK(aK + sl, i);
      phaseA(sa, sb, true, false, true);
      mx = sa[0][0];
#pragma unroll
      for (int i = 0; i < 4; ++i) mx = fmaxf(fmaxf(fmaxf(sa[i][0], sa[i][1]), mx), fmaxf(sa[i][2], sa[i][3]));
      shift(sa, xmax4(mx));
#pragma unroll 1
      for (int j = 0; j < 14; j += 2) {
        phaseA(sb, sa, true, true, false);
        if (j == 6) rdQ(1);
        phaseB(sb, true, true); check(sb);
        phaseA(sa, sb, true, true, false); phaseB(sa, true, true);
        if (j == 6) { finish(0); shift(sa, xmax4(mx)); mx = 0.f; }
        check(sa);
      }
      phaseA(sb, sa, true, true, false); phaseB(sb, true, false); check(sb);
      phaseA(sa, sb, false, true, false); phaseB(sa, false, false);
      finish(1);
    }
    {
      const unsigned l = fresh(lane), ao = L0 + RING + ((l >> 4) & 1) * 16384 + (l >> 5) * 256 + (l & 15) * 16;
      gemm([&](unsigned ks) INL { return ao + ks * 1024; }, 512, 32768, false);
    }
#pragma unroll
    for (int i = 0; i < 8; ++i) {
      const f32x16& v = acc[i >> 2][(i >> 1) & 1];
      const int r0 = (i & 1) * 8;
      __builtin_amdgcn_raw_buffer_store_b128(cvt8(f32x4{v[r0], v[r0 + 1], v[r0 + 2], v[r0 + 3]}, f32x4{v[r0 + 4], v[r0 + 5], v[r0 + 6], v[r0 + 7]}), rt, lane * 16,
                                             WS_TILES_A + ((2 * idx + ((i >> 1) & 1)) * 8 + xcd) * TILE_B + (((i >> 2) * 8 + cw) * 2 + (i & 1)) * 1024, 16);
    }
    asm volatile("s_waitcnt vmcnt(0)\n\ts_barrier" ::: "memory");
    if (cw == 0 && lane < 16) __hip_atomic_store((GAS u64*)(a.ws + WS_FLAG + ((2 * idx * 8 + lane) * 32 + 16 + xcd) * 8), tag, RLX);
  } else {
    unsigned aq, ax0;
    {
      const unsigned l = fresh(lane), g = l >> 4, qq = (l >> 2) & 3;
      aq = L0 + (8 * g + qq) * 256 + (((unsigned)cw ^ (qq ^ (4 * (g & 1)))) << 5) + 8 * (l & 3);
      ax0 = AK();
    }
    const unsigned al = AL() + poff, aF = AF() + cw * 512 + poff;
    f32x4 qa[2];
    u32x4 wf[2][2], xf[2][4];
    auto qrd = [&](int st, int p) INL {
      const unsigned ii = (st + bB) & 7, b = aq + sl, ax = (ax0 + (RING + (ii >> 1) * 8192)) ^ ((ii & 1) << 7);
#pragma unroll
      for (int ks = 0; ks < 2; ++ks) wf[p][ks] = ldtr(b + ks * 8192, (b ^ 128) + 1024 + ks * 8192);
#pragma unroll
      for (int i = 0; i < 4; ++i) xf[p][i] = ld128((ax ^ ((i >> 1) << 6)) + (i & 1) * 4096);
      sl = nxt(sl);
      SB();
    };
    auto qmm = [&](int p) INL {
#pragma unroll
      for (int i = 0; i < 4; ++i) qa[i & 1] = mfma(wf[p][i >> 1], xf[p][i], qa[i & 1]);
      SB();
    };
    auto qrest = [&]() INL {
#pragma unroll 1
      for (int st = 1; st < 7; st += 2) {
        rbar(); qrd(st, 1); qmm(0);
        rbar(); qrd(st + 1, 0); qmm(1);
      }
      rbar(); qrd(7, 1); qmm(0);
    };
    auto qwr = [&]() INL {
#pragma unroll
      for (int qb = 0; qb < 2; ++qb) st64(aF + Q_OFF + qb * 4096, cvt4(qa[qb]));
      SB();
    };
    qa[0] = Z4; qa[1] = Z4;
    rbar(); qrd(0, 0); qrest(); qmm(1); qwr();
    mktag();
    cbar();
    f32x4 oa[4][2];
#pragma unroll
    for (int nb = 0; nb < 4; ++nb) { oa[nb][0] = Z4; oa[nb][1] = Z4; }
#pragma unroll 1
    for (int u = 0; u < nu; ++u) {
      f32x4 s[8];
      float mx = -INFINITY;
      const unsigned aS = L0 + RING + poff + ST_OFF + (fresh(lane) & 15) * 16;
      const unsigned aP = al + ((cw >> 2) * 2048 + (cw & 1) * 1024 + ((cw >> 1) & 1) * 8);
      const unsigned aV = AV() ^ ((unsigned)cw << 5), aw = AW();
      {
        u32x4 qf[4], kf[2][4];
        const unsigned aK = ax0 + (cw >> 1) * 4096;
        auto kmm = [&](int i) INL {
          s[i] = Z4;
#pragma unroll
          for (int kk = 0; kk < 4; ++kk) s[i] = mfma(kf[i & 1][kk], qf[kk], s[i]);
          if (i > 0) mx = fmaxf(fmaxf(fmaxf(s[i - 1][0], s[i - 1][1]), mx), fmaxf(s[i - 1][2], s[i - 1][3]));
          SB();
        };
#pragma unroll
        for (int i = 0; i < 8; ++i) {
          if (i > 0 || u > 0) rbar();
          const unsigned b = aK + sl;
#pragma unroll
          for (int kk = 0; kk < 4; ++kk) kf[i & 1][kk] = ld128(b ^ (kk << 6));
          if (i == 0) {
#pragma unroll
            for (int kk = 0; kk < 4; ++kk) qf[kk] = ld128(al + (Q_OFF + (cw & 1) * 4096) + kk * 1024);
          }
          sl = nxt(sl);
          SB();
          if (i > 0 && i != 6) kmm(i - 1);
          if (i == 5) {
            kmm(5);
            if (((dfm << 1) >> u) & 1) {
#pragma unroll
              for (int t = 0; t <= 8; ++t) {
                if (t < 8) {
                  rbar();
                  const unsigned bw = aw + sl;
#pragma unroll
                  for (int nb = 0; nb < 2; ++nb) wf[t & 1][nb] = ldtr(bw ^ (nb << 5), (bw ^ (nb << 5) ^ 128) + 2048);
                  if (!(t & 1)) {
#pragma unroll
                    for (int qb = 0; qb < 2; ++qb) xf[(t >> 1) & 1][qb] = ld128(al + (((t >> 1) + bB) & 3) * 1024 + qb * 4096);
                  }
                  sl = nxt(sl);
                  SB();
                }
                if (t > 0) {
                  const int p = t - 1;
#pragma unroll
                  for (int j = 0; j < 4; ++j) oa[(p & 1) * 2 + (j >> 1)][j & 1] = mfma(wf[p & 1][j >> 1], xf[(p >> 1) & 1][j & 1], oa[(p & 1) * 2 + (j >> 1)][j & 1]);
                  SB();
                }
              }
            }
            if (((eqm >> u) & 1) && u + 1 < nu) {
              qa[0] = Z4; qa[1] = Z4;
              rbar(); qrd(0, 0); qrest(); qmm(1); qwr();
            }
          }
        }
        kmm(7);
      }
      mx = xmax4(fmaxf(fmaxf(fmaxf(s[7][0], s[7][1]), mx), fmaxf(s[7][2], s[7][3])));
      if (lane < 16) *(LAS float*)(uintptr_t)(aS + (cw & 1) * 256 + (cw >> 1) * 4) = mx;
      cbar();
      const f32x4 m0 = ldf4(aS + (cw & 1) * 256);
      const float mn = SL2E * fmaxf(fmaxf(m0[0], m0[1]), fmaxf(m0[2], m0[3]));
      f32x4 ps = Z4;
      auto pexp = [&](int i) INL {
        const f32x4 e = s[i] * SL2E - mn;
        f32x4 p;
#pragma unroll
        for (int r = 0; r < 4; ++r) p[r] = __builtin_amdgcn_exp2f(e[r]);
        ps += p;
        st64(aP + (i & 3) * 4096, cvt4(p));
        SB();
      };
      pexp(0); pexp(1);
      f32x4 o[2] = {Z4, Z4};
      u32x4 vf[2][2], pf[2][4];
      auto vrd = [&](int i) INL {
        const unsigned b = aV + sl;
#pragma unroll
        for (int ks = 0; ks < 2; ++ks) vf[i & 1][ks] = ldtr(b + ks * 8192, b + ks * 8192 + 4096);
#pragma unroll
        for (int j = 0; j < 4; ++j) pf[i & 1][j] = ld128(al + ((i & 3) * 4096 + j * 1024));
        sl = nxt(sl);
        SB();
      };
      auto vmm = [&](int i) INL {
#pragma unroll
        for (int j = 0; j < 4; ++j) o[j & 1] = mfma(vf[i & 1][j >> 1], pf[i & 1][j], o[j & 1]);
        SB();
      };
#pragma unroll
      for (int i = 0; i < 6; ++i) {
        if (i == 0) cbar(); else { SB(); asm volatile("s_waitcnt lgkmcnt(9)\n\ts_barrier" ::: "memory"); SB(); }
        vrd(i);
        if (i > 0) vmm(i - 1);
        pexp(i + 2);
      }
      {
        const float pt = xsum4((ps[0] + ps[1]) + (ps[2] + ps[3]));
        if (lane < 16) *(LAS float*)(uintptr_t)(aS + 512 + (cw & 1) * 256 + (cw >> 1) * 4) = pt;
        SB();
      }
      auto vtail = [&]() INL {
        cbar(); vrd(6);
        float inv[2];
#pragma unroll
        for (int qb = 0; qb < 2; ++qb) { const f32x4 l0 = ldf4(aS + 512 + qb * 256); inv[qb] = __builtin_amdgcn_rcpf((l0[0] + l0[1]) + (l0[2] + l0[3])); }
        vmm(6);
        rbar(); vrd(7); vmm(7);
#pragma unroll
        for (int qb = 0; qb < 2; ++qb) st64(aF + qb * 4096, cvt4(o[qb] * inv[qb]));
        cbar();
      };
      const bool lateq = !((eqm >> u) & 1) && u + 1 < nu;
      int dfu = dfm >> u;
      if (dfu & 1) {
        if (lateq) { qa[0] = Z4; qa[1] = Z4; rbar(); qrd(0, 0); vmm(5); qrest(); qmm(1); qwr(); } else vmm(5);
        vtail();
      }
      asm volatile("" : "+s"(dfu));
      if (!(dfu & 1)) {
        u32x4 wo[4][4];
        auto wrd = [&](int i) INL {
          const unsigned b = aw + sl;
#pragma unroll
          for (int nb = 0; nb < 2; ++nb) wo[i >> 1][(i & 1) * 2 + nb] = ldtr(b ^ (nb << 5), (b ^ (nb << 5) ^ 128) + 2048);
          sl = nxt(sl);
          SB();
        };
        if (lateq) {
          qa[0] = Z4; qa[1] = Z4;
          rbar(); qrd(0, 0); vmm(5);
          qrest();
          rbar(); wrd(0); qmm(1);
          rbar(); wrd(1); qwr();
        } else {
          rbar(); wrd(0); vmm(5);
          rbar(); wrd(1);
        }
#pragma unroll
        for (int i = 2; i < 8; ++i) {
          SB(); asm volatile("s_waitcnt lgkmcnt(4)\n\ts_barrier" ::: "memory"); SB();
          wrd(i);
        }
        vtail();
        u32x4 of[2][4];
#pragma unroll
        for (int qb = 0; qb < 2; ++qb)
#pragma unroll
          for (int kk = 0; kk < 4; ++kk) of[qb][kk] = ld128(al + ((kk + bB) & 3) * 1024 + qb * 4096);
        SB();
#pragma unroll
        for (int qb = 0; qb < 2; ++qb)
#pragma unroll
          for (int kk = 0; kk < 4; ++kk)
#pragma unroll
            for (int nb = 0; nb < 4; ++nb) oa[nb][qb] = mfma(wo[kk][nb], of[qb][kk], oa[nb][qb]);
      }
    }
#pragma unroll
    for (int i = 0; i < 4; ++i)
      __builtin_amdgcn_raw_buffer_store_b128(cvt8(oa[(i & 1) * 2][i >> 1], oa[(i & 1) * 2 + 1][i >> 1]), rt, lane * 16, (bB * 12 + slotB) * TILE_B + (((i & 1) * 8 + cw) * 2 + (i >> 1)) * 1024, 16);
    asm volatile("s_waitcnt vmcnt(0)\n\ts_barrier" ::: "memory");
    if (cw == 0 && lane < 8) __hip_atomic_store((GAS u64*)(a.ws + WS_FLAG + ((bB * 8 + lane) * 32 + slotB) * 8), tag, RLX);
  }

  {
    const int rb = blockIdx.x >> 4, pj = blockIdx.x & 15, pc = pj * 2 + ((cw >> 1) & 1), hb = cw & 1;
    const bool wB = cw < 4;
    const GAS u64* fp = (const GAS u64*)(a.ws + WS_FLAG + ((rb * 8 + (pj & 7)) * 32 + (wB ? (lane < 12 ? lane : 0) : 16 + (lane & 7))) * 8);
    for (int spin = 0; spin < NSPIN; ++spin) {
      if (__all(__hip_atomic_load(fp, RLX) == tag)) break;
      __builtin_amdgcn_s_sleep(2);
    }
    __builtin_amdgcn_fence(__ATOMIC_ACQUIRE, "wavefront");
    const unsigned so = (wB ? rb * 12 * TILE_B : WS_TILES_A + rb * 8 * TILE_B) + pc * 1024 + hb * 512;
    const unsigned vo = (lane >> 5) * TILE_B + (lane & 31) * 16;
    u32x4 t[6];
#pragma unroll
    for (int i = 0; i < 6; ++i) t[i] = __builtin_amdgcn_raw_buffer_load_b128(rt, (i < 4 || wB) ? vo : 0x7fffff00u, so + i * 2 * TILE_B, 16);
    f32x4 s0 = Z4, s1 = Z4;
#pragma unroll
    for (int i = 0; i < 6; ++i) { s0 += bf4(t[i][0], t[i][1]); s1 += bf4(t[i][2], t[i][3]); }
    f32x4 r;
#pragma unroll
    for (int j = 0; j < 4; ++j) {
      const auto x = __builtin_amdgcn_permlane32_swap(__float_as_uint(s0[j]), __float_as_uint(s1[j]), false, false);
      r[j] = __uint_as_float(x[0]) + __uint_as_float(x[1]);
    }
    const unsigned sl_ = hb * 32 + (lane & 31);
    const unsigned row = wB ? (pc & 1) * 16 + (sl_ & 15) : (lane & 31);
    const unsigned col = wB ? 512 + 16 * (lane >> 5) + 4 * (sl_ >> 4) : 16 * (pc & 1) + 8 * (lane >> 5) + 4 * hb;
    *(GAS u32x2*)(a.out + ((size_t)(rb * SQ + row) * 1024 + 32 * (pc >> 1) + col) * 2) = cvt4(r);
  }
}

extern "C" void kernel_launch(void* const* d_in, const int* in_sizes, int n_in, void* d_out, int out_size, void* d_ws, size_t ws_size,
                              hipStream_t stream) {
  Args a;
  for (int br = 0; br < 2; ++br) {
    a.x[br] = d_in[5 * br + 0];
    a.k[br] = d_in[5 * br + 1];
    a.v[br] = d_in[5 * br + 2];
    a.wq[br] = d_in[5 * br + 3];
    a.wo[br] = d_in[5 * br + 4];
  }
  a.ws = (char*)d_ws;
  a.out = (char*)d_out;
  (void)hipFuncSetAttribute((const void*)fused_k, hipFuncAttributeMaxDynamicSharedMemorySize, LDS_BYTES);
  fused_k<<<256, 768, LDS_BYTES, stream>>>(a);
}
```
